```python
import torch

def _softmax(x, axis=-1):
    return torch.softmax(x, dim=axis)
def _gelu(x):
    return torch.nn.functional.gelu(x, approximate="tanh")
def _silu(x):
    return torch.nn.functional.silu(x)
def _sigmoid(x):
    return torch.sigmoid(x)
def _layernorm(x, axis=-1, eps=1e-5):
    m = x.mean(dim=axis, keepdim=True)
    v = x.var(dim=axis, keepdim=True, unbiased=False)
    return (x - m) / torch.sqrt(v + eps)
def _scatter_add(tgt, idx, src, axis=0):
    return tgt.clone().index_add_(axis, idx, src)
def _segment_sum(src, seg, n):
    out = torch.zeros((n,) + src.shape[1:], dtype=src.dtype)
    return out.index_add_(0, seg, src)
def _pad(x, pw):
    flat = tuple(v for pair in reversed(pw) for v in pair)
    return torch.nn.functional.pad(x, flat)

kernel_name = 'standalone__dense_activation_relu__prompt'

def setup_inputs():

    _w = (lambda a: (lambda t: t.to(torch.bfloat16) if t.is_floating_point() else t)(torch.as_tensor(a)))
    B, S, D, Dff = 1, 2048, 448, 1792
    return {
        "x": _w((lambda *s: torch.randn(*s, dtype=torch.bfloat16) * 0.05)(B, S, D)),
        "w1": _w((lambda *s: torch.randn(*s, dtype=torch.bfloat16) * 0.05)(D, Dff)),
        "w2": _w((lambda *s: torch.randn(*s, dtype=torch.bfloat16) * 0.05)(Dff, D)),
    }

def reference(x, w1, w2):
    h = torch.einsum("bsd,df->bsf", x, w1)
    zero = torch.zeros_like(h)
    h = torch.maximum(h, zero)
    return torch.einsum("bsf,fd->bsd", h, w2)
```

```cpp
#include <hip/hip_runtime.h>
#include <hip/hip_bf16.h>
typedef short bf16x8 __attribute__((ext_vector_type(8)));
typedef float f32x4 __attribute__((ext_vector_type(4)));
typedef unsigned short u16;

template <bool RELU>
__global__ __launch_bounds__(256) void gemm_simple(const u16* __restrict__ A, const u16* __restrict__ B, u16* __restrict__ C, int M, int N, int K) {
  __shared__ u16 As[64][40];
  __shared__ u16 Bs[64][40];
  const int tid = threadIdx.x, lane = tid & 63, w = tid >> 6;
  const int bm = blockIdx.y * 64, bn = blockIdx.x * 64;
  const int wr = w >> 1, wc = w & 1;
  f32x4 acc[2][2] = {};
  for (int k0 = 0; k0 < K; k0 += 32) {
    {
      int r = tid >> 2, c = (tid & 3) * 8;
      bf16x8 v = *(const bf16x8*)(A + (size_t)(bm + r) * K + k0 + c);
      *(bf16x8*)&As[r][c] = v;
      int kr = tid >> 3, nc = (tid & 7) * 8;
      bf16x8 b = *(const bf16x8*)(B + (size_t)(k0 + kr) * N + bn + nc);
      for (int j = 0; j < 8; ++j) Bs[nc + j][kr] = (u16)b[j];
    }
    __syncthreads();
    bf16x8 a[2], b[2];
    for (int i = 0; i < 2; ++i) {
      a[i] = *(const bf16x8*)&As[wr * 32 + i * 16 + (lane & 15)][(lane >> 4) * 8];
      b[i] = *(const bf16x8*)&Bs[wc * 32 + i * 16 + (lane & 15)][(lane >> 4) * 8];
    }
    for (int i = 0; i < 2; ++i)
      for (int j = 0; j < 2; ++j) acc[i][j] = __builtin_amdgcn_mfma_f32_16x16x32_bf16(a[i], b[j], acc[i][j], 0, 0, 0);
    __syncthreads();
  }
  for (int i = 0; i < 2; ++i)
    for (int j = 0; j < 2; ++j)
      for (int r = 0; r < 4; ++r) {
        float v = acc[i][j][r];
        if (RELU) v = fmaxf(v, 0.f);
        __hip_bfloat16 h = __float2bfloat16(v);
        C[(size_t)(bm + wr * 32 + i * 16 + (lane >> 4) * 4 + r) * N + bn + wc * 32 + j * 16 + (lane & 15)] = *(u16*)&h;
      }
}

extern "C" void kernel_launch(void* const* d_in, const int* in_sizes, int n_in, void* d_out, int out_size, void* d_ws, size_t ws_size, hipStream_t stream) {
  const u16* x = (const u16*)d_in[0];
  const u16* w1 = (const u16*)d_in[1];
  const u16* w2 = (const u16*)d_in[2];
  u16* h = (u16*)d_ws;
  gemm_simple<true><<<dim3(1792 / 64, 2048 / 64), 256, 0, stream>>>(x, w1, h, 2048, 1792, 448);
  gemm_simple<false><<<dim3(448 / 64, 2048 / 64), 256, 0, stream>>>(h, w2, (u16*)d_out, 2048, 448, 1792);
}
```

```cpp
#include <hip/hip_runtime.h>
#include <hip/hip_bf16.h>

__global__ __launch_bounds__(256, 1) void ffn_fused_kernel(const void* x, unsigned xb, unsigned xf, const void* w1, unsigned w1b, unsigned w1f,
                                                           const void* w2, unsigned w2b, unsigned w2f, void* ws, unsigned wsb, unsigned wsf,
                                                           void* out, unsigned ob, unsigned of) {
  __shared__ __attribute__((aligned(16))) char smem[163840];
  asm volatile(
    ".Lstart%=:\n"
    "s_load_dwordx16 s[36:51], %0, 0x0\n"
    "v_lshrrev_b32 v54, 6, %4\n"
    "v_and_b32 v1, 63, %4\n"
    "v_readfirstlane_b32 s16, v54\n"
    "s_and_b32 s17, %1, 1\n"
    "s_lshr_b32 s18, %1, 1\n"
    "s_lshl2_add_u32 s18, s18, %2\n"
    "s_cmp_eq_u32 %3, 7\n"
    "s_cbranch_scc1 .Lspare%=\n"
    "v_lshrrev_b32 v54, 3, v1\n"
    "v_and_b32 v55, 7, v1\n"
    "v_lshrrev_b32 v56, 4, v1\n"
    "v_mul_u32_u24 v52, 896, v54\n"
    "s_add_u32 s22, %2, %3\n"
    "s_and_b32 s22, s22, 1\n"
    "s_lshr_b32 s57, %1, 1\n"
    "s_lshl2_add_u32 s22, s22, s57\n"
    "s_lshl1_add_u32 s22, s17, s22\n"
    "s_lshl_b32 s56, s17, 1\n"
    "s_sub_u32 s57, s22, 7\n"
    "s_cselect_b32 s22, s22, s57\n"
    "s_xor_b32 s56, s56, s16\n"
    "s_lshl2_add_u32 s56, s18, s56\n"
    "s_mul_i32 s23, s56, 28672\n"
    "s_lshl_b32 s28, s16, 12\n"
    "v_xor_b32 v60, v55, v56\n"
    "v_lshl_add_u32 v2, v60, 4, v52\n"
    "v_xor_b32 v3, 64, v2\n"
    "v_add_u32 v4, 12288, v2\n"
    "v_add_u32 v5, 18432, v3\n"
    "v_add_u32 v3, 6144, v3\n"
    "s_mul_i32 s19, s17, 7\n"
    "v_and_b32 v60, 15, v1\n"
    "s_add_u32 s19, s19, %3\n"
    "v_lshlrev_b32 v61, 2, v56\n"
    "s_mul_i32 s24, s16, 57344\n"
    "v_xor_b32 v60, v60, v61\n"
    "s_lshl_b32 s56, s19, 8\n"
    "v_mul_u32_u24 v61, 3584, v56\n"
    "s_add_u32 s24, s24, s56\n"
    "v_lshl_add_u32 v6, v60, 4, v61\n"
    "v_xor_b32 v8, 32, v6\n"
    "v_add_u32 v7, 13312, v6\n"
    "v_add_u32 v9, 39936, v8\n"
    "v_add_u32 v8, 26624, v8\n"
    "s_lshl_b32 s20, s22, 7\n"
    "s_add_u32 m0, s28, 0\n"
    "s_add_u32 s20, s20, s23\n"
    "s_waitcnt lgkmcnt(0)\n"
    "; ---- descriptors loaded: first x / w1 chunk\n"
    "buffer_load_dwordx4 v2, s[36:39], s20 offen offset:0 lds\n"
    "buffer_load_dwordx4 v3, s[36:39], s20 offen offset:1024 lds\n"
    "buffer_load_dwordx4 v4, s[36:39], s20 offen offset:2048 lds\n"
    "buffer_load_dwordx4 v5, s[36:39], s20 offen offset:3072 lds\n"
    "s_mul_i32 s21, s22, 229376\n"
    "s_add_u32 m0, s28, 16384\n"
    "s_add_u32 s21, s21, s24\n"
    "s_add_u32 s22, s22, 1\n"
    "s_cmp_eq_u32 s22, 7\n"
    "s_cselect_b32 s22, 0, s22\n"
    "buffer_load_dwordx4 v6, s[40:43], s21 offen offset:0 lds\n"
    "buffer_load_dwordx4 v7, s[40:43], s21 offen offset:1024 lds\n"
    "buffer_load_dwordx4 v8, s[40:43], s21 offen offset:2048 lds\n"
    "buffer_load_dwordx4 v9, s[40:43], s21 offen offset:3072 lds\n"
    "s_load_dwordx4 s[52:55], %0, 0x40\n"
    "s_lshl2_add_u32 s62, s18, s16\n"
    "s_lshl_b32 s62, s62, 13\n"
    "s_add_u32 s62, s62, 0xc40000\n"
    "s_add_u32 s61, s62, 1920\n"
    "buffer_load_dwordx2 v[38:39], off, s[48:51], s61 sc1\n"
    "s_lshl_b32 s20, s22, 7\n"
    "s_add_u32 m0, s28, 32768\n"
    "s_add_u32 s20, s20, s23\n"
    "buffer_load_dwordx4 v2, s[36:39], s20 offen offset:0 lds\n"
    "buffer_load_dwordx4 v3, s[36:39], s20 offen offset:1024 lds\n"
    "s_lshl_b32 s60, s19, 7\n"
    "s_add_u32 s60, s60, s62\n"
    "s_lshl_b32 s29, s16, 13\n"
    "v_and_b32 v57, 3, v1\n"
    "v_bfe_u32 v58, v1, 2, 2\n"
    "v_and_b32 v59, 15, v1\n"
    "v_lshrrev_b32 v61, 1, v59\n"
    "v_xor_b32 v61, v61, v56\n"
    "v_lshlrev_b32 v61, 4, v61\n"
    "v_lshl_add_u32 v14, v59, 7, v61\n"
    "v_xor_b32 v15, 64, v14\n"
    "v_add_u32 v16, 65536, v14\n"
    "v_add_u32 v17, 65536, v15\n"
    "v_add_u32 v18, 131072, v14\n"
    "v_add_u32 v19, 131072, v15\n"
    "v_lshl_add_u32 v61, v56, 3, v58\n"
    "v_and_b32 v40, 1, v56\n"
    "v_lshl_add_u32 v40, v58, 1, v40\n"
    "s_lshl_b32 s56, s16, 1\n"
    "v_xor_b32 v40, s56, v40\n"
    "v_lshlrev_b32 v40, 5, v40\n"
    "v_lshl_add_u32 v40, v61, 8, v40\n"
    "v_lshl_add_u32 v20, v57, 3, v40\n"
    "v_xor_b32 v21, 32, v20\n"
    "v_add_u32 v22, 65536, v20\n"
    "v_add_u32 v23, 65536, v21\n"
    "v_add_u32 v24, 131072, v20\n"
    "v_add_u32 v25, 131072, v21\n"
    "s_lshl_b32 s56, s22, 7\n"
    "s_add_u32 s56, s56, 128\n"
    "s_cmp_eq_u32 s56, 896\n"
    "s_cselect_b32 s56, 0, s56\n"
    "s_add_u32 s6, s56, s23\n"
    "s_mul_i32 s11, s56, 1792\n"
    "s_add_u32 s11, s11, s24\n"
    "s_add_u32 s56, s56, 128\n"
    "s_cmp_eq_u32 s56, 896\n"
    "s_cselect_b32 s56, 0, s56\n"
    "s_add_u32 s7, s56, s23\n"
    "s_mul_i32 s12, s56, 1792\n"
    "s_add_u32 s12, s12, s24\n"
    "s_add_u32 s56, s56, 128\n"
    "s_cmp_eq_u32 s56, 896\n"
    "s_cselect_b32 s56, 0, s56\n"
    "s_add_u32 s8, s56, s23\n"
    "s_mul_i32 s13, s56, 1792\n"
    "s_add_u32 s13, s13, s24\n"
    "s_add_u32 s56, s56, 128\n"
    "s_cmp_eq_u32 s56, 896\n"
    "s_cselect_b32 s56, 0, s56\n"
    "s_add_u32 s9, s56, s23\n"
    "s_mul_i32 s14, s56, 1792\n"
    "s_add_u32 s14, s14, s24\n"
    "s_add_u32 s56, s56, 128\n"
    "s_cmp_eq_u32 s56, 896\n"
    "s_cselect_b32 s56, 0, s56\n"
    "s_add_u32 s10, s56, s23\n"
    "s_mul_i32 s15, s56, 1792\n"
    "s_add_u32 s15, s15, s24\n"
    "v_lshl_add_u32 v61, v56, 2, v58\n"
    "v_bfe_u32 v40, v61, 1, 2\n"
    "v_lshlrev_b32 v40, 5, v40\n"
    "v_lshl_add_u32 v40, v61, 7, v40\n"
    "v_lshl_add_u32 v40, v57, 3, v40\n"
    "v_add_u32 v26, s29, v40\n"
    "v_xor_b32 v27, 32, v26\n"
    "v_xor_b32 v28, 64, v26\n"
    "v_xor_b32 v29, 96, v26\n"
    "v_add_u32 v30, 65536, v26\n"
    "v_add_u32 v31, 65536, v27\n"
    "v_add_u32 v32, 65536, v28\n"
    "v_add_u32 v33, 65536, v29\n"
    "v_add_u32 v34, 131072, v26\n"
    "v_add_u32 v35, 131072, v27\n"
    "v_add_u32 v36, 131072, v28\n"
    "v_add_u32 v37, 131072, v29\n"
    "v_lshl_add_u32 v52, v55, 4, v52\n"
    "v_lshlrev_b32 v40, 5, v56\n"
    "v_xor_b32 v10, v52, v40\n"
    "s_lshl_b32 s59, %3, 7\n"
    "s_mul_i32 s56, s16, 28672\n"
    "s_add_u32 s56, s56, s59\n"
    "v_add_u32 v10, s56, v10\n"
    "v_add_u32 v11, 6144, v10\n"
    "v_add_u32 v12, 12288, v10\n"
    "v_add_u32 v13, 18432, v10\n"
    "v_mul_u32_u24 v53, 896, v56\n"
    "v_lshl_add_u32 v53, v59, 3, v53\n"
    "v_lshlrev_b32 v61, 1, v55\n"
    "v_xor_b32 v61, v61, v54\n"
    "v_lshlrev_b32 v61, 4, v61\n"
    "v_lshl_add_u32 v61, v54, 8, v61\n"
    "s_lshl_b32 s58, s16, 11\n"
    "s_add_u32 s56, s58, 65536\n"
    "v_add_u32 v49, s56, v61\n"
    "v_xor_b32 v50, 16, v49\n"
    "s_lshl_b32 s56, s16, 2\n"
    "v_add_u32 v40, s56, v56\n"
    "v_and_b32 v41, 7, v40\n"
    "v_xor_b32 v41, v41, v59\n"
    "v_lshlrev_b32 v41, 4, v41\n"
    "v_lshl_add_u32 v40, v40, 8, v41\n"
    "v_add_u32 v51, 65536, v40\n"
    "v_xor_b32 v61, v55, v56\n"
    "v_lshlrev_b32 v61, 4, v61\n"
    "v_lshl_add_u32 v61, v59, 8, v61\n"
    "s_add_u32 s56, s29, 65536\n"
    "v_add_u32 v47, s56, v61\n"
    "v_xor_b32 v48, 64, v47\n"
    "s_lshr_b32 s56, s18, 1\n"
    "buffer_load_dwordx4 v4, s[36:39], s20 offen offset:2048 lds\n"
    "buffer_load_dwordx4 v5, s[36:39], s20 offen offset:3072 lds\n"
    "s_mul_i32 s21, s22, 229376\n"
    "s_add_u32 m0, s28, 49152\n"
    "s_add_u32 s21, s21, s24\n"
    "buffer_load_dwordx4 v6, s[40:43], s21 offen offset:0 lds\n"
    "buffer_load_dwordx4 v7, s[40:43], s21 offen offset:1024 lds\n"
    "buffer_load_dwordx4 v8, s[40:43], s21 offen offset:2048 lds\n"
    "buffer_load_dwordx4 v9, s[40:43], s21 offen offset:3072 lds\n"
    "s_add_u32 m0, s28, 65536\n"
    "s_nop 0\n"
    "buffer_load_dwordx4 v2, s[36:39], s6 offen offset:0 lds\n"
    "buffer_load_dwordx4 v3, s[36:39], s6 offen offset:1024 lds\n"
    "s_mul_i32 s56, s56, 1605632\n"
    "s_and_b32 s57, s18, 1\n"
    "s_lshl1_add_u32 s57, s57, s17\n"
    "s_lshl3_add_u32 s57, s57, s16\n"
    "s_lshl_b32 s57, s57, 11\n"
    "s_add_u32 s56, s56, s57\n"
    "v_lshl_add_u32 v42, v1, 4, s56\n"
    "v_add_u32 v43, 8192, v42\n"
    "v_xor_b32 v44, 16384, v42\n"
    "v_xor_b32 v45, 16384, v43\n"
    "s_lshl1_add_u32 s56, s18, s17\n"
    "s_lshl3_add_u32 s56, s56, s16\n"
    "s_mul_i32 s63, s56, 7168\n"
    "s_add_u32 s63, s63, s59\n"
    "s_mul_i32 s57, s16, 3584\n"
    "s_sub_u32 s65, s63, s57\n"
    "s_add_u32 s65, s65, 28672\n"
    "s_mul_i32 s56, %1, 7\n"
    "s_lshr_b32 s56, s56, 2\n"
    "s_sub_u32 s57, s19, s56\n"
    "s_cselect_b32 s58, 14, 0\n"
    "s_add_u32 s57, s57, s58\n"
    "v_cmp_le_u32 vcc, s57, v1\n"
    "v_mov_b32 v41, s56\n"
    "s_mul_i32 s66, s19, 114688\n"
    "v_addc_co_u32 v40, vcc, v1, v41, vcc\n"
    "v_subrev_u32 v41, 14, v40\n"
    "v_min_u32 v40, v40, v41\n"
    "v_mul_u32_u24 v41, 114688, v40\n"
    "v_readfirstlane_b32 s56, v40\n"
    "v_cmp_gt_u32 vcc, 13, v1\n"
    "v_readlane_b32 s67, v41, 0\n"
    "v_readlane_b32 s68, v41, 1\n"
    "v_readlane_b32 s69, v41, 2\n"
    "v_readlane_b32 s70, v41, 3\n"
    "v_readlane_b32 s71, v41, 4\n"
    "v_readlane_b32 s72, v41, 5\n"
    "v_readlane_b32 s73, v41, 6\n"
    "v_readlane_b32 s74, v41, 7\n"
    "v_readlane_b32 s75, v41, 8\n"
    "v_readlane_b32 s76, v41, 9\n"
    "v_readlane_b32 s77, v41, 10\n"
    "v_readlane_b32 s78, v41, 11\n"
    "v_readlane_b32 s79, v41, 12\n"
    "v_mov_b32 v54, s56\n"
    "v_cndmask_b32 v46, v54, v40, vcc\n"
    "v_lshlrev_b32 v46, 7, v46\n"
    "v_lshlrev_b32 v63, 4, v1\n"
    "s_branch .Lpage1_%=\n"
    ".Lpoll_retry0_%=:\n"
    "s_mov_b32 s64, 0x20000\n"
    ".Lpoll_loop0_%=:\n"
    "buffer_load_dwordx2 v[40:41], v46, s[48:51], s62 offen offset:0 sc1\n"
    "s_waitcnt vmcnt(0)\n"
    "v_cmp_eq_u64 vcc, v[40:41], v[56:57]\n"
    "s_cmp_eq_u64 vcc, exec\n"
    "s_cbranch_scc1 .Lpoll_ok0_%=\n"
    "s_sub_u32 s64, s64, 1\n"
    "s_cbranch_scc0 .Lpoll_loop0_%=\n"
    "s_branch .Lpoll_ok0_%=\n"
    ".Lpoll_retry1_%=:\n"
    "s_mov_b32 s64, 0x20000\n"
    ".Lpoll_loop1_%=:\n"
    "buffer_load_dwordx2 v[40:41], v46, s[48:51], s62 offen offset:2048 sc1\n"
    "s_waitcnt vmcnt(0)\n"
    "v_cmp_eq_u64 vcc, v[40:41], v[56:57]\n"
    "s_cmp_eq_u64 vcc, exec\n"
    "s_cbranch_scc1 .Lpoll_ok1_%=\n"
    "s_sub_u32 s64, s64, 1\n"
    "s_cbranch_scc0 .Lpoll_loop1_%=\n"
    "s_branch .Lpoll_ok1_%=\n"
    "; ---- prefetch workgroups\n"
    ".Lspare%=:\n"
    "s_cmp_lg_u32 %2, 0\n"
    "s_cbranch_scc1 .Lnocpf%=\n"
    "s_getpc_b64 s[66:67]\n"
    ".Lpc%=:\n"
    "v_lshlrev_b32 v56, 7, %4\n"
    "v_min_u32 v56, .Lend%=-.Lpc%=-4, v56\n"
    "global_load_dword v103, v56, s[66:67]\n"
    ".Lnocpf%=:\n"
    "s_lshl2_add_u32 s21, s16, %2\n"
    "v_lshlrev_b32 v54, 7, v1\n"
    "v_mul_u32_u24 v55, 896, v1\n"
    "s_waitcnt lgkmcnt(0)\n"
    "s_lshr_b32 s56, %1, 1\n"
    "s_and_b32 s57, s21, 1\n"
    "s_lshl2_add_u32 s57, s57, s56\n"
    "s_lshl1_add_u32 s57, s17, s57\n"
    "s_add_u32 s57, s57, 1\n"
    "s_sub_u32 s58, s57, 7\n"
    "s_cselect_b32 s57, s57, s58\n"
    "s_lshl_b32 s57, s57, 7\n"
    "s_lshr_b32 s58, s21, 1\n"
    "s_lshl3_add_u32 s56, s56, s58\n"
    "s_mul_i32 s56, s56, 57344\n"
    "s_add_u32 s56, s56, s57\n"
    "buffer_load_dword v64, v55, s[36:39], s56 offen\n"
    "s_mul_i32 s56, %1, 7\n"
    "s_lshr_b32 s56, s56, 2\n"
    "s_mul_i32 s56, s56, 14\n"
    "s_add_u32 s56, s56, s21\n"
    "s_sub_u32 s59, s56, 196\n"
    "s_cselect_b32 s56, s56, s59\n"
    "s_lshl_b32 s58, s56, 13\n"
    "buffer_load_dword v65, v54, s[44:47], s58 offen\n"
    "s_add_u32 s56, s56, 16\n"
    "s_sub_u32 s59, s56, 196\n"
    "s_cselect_b32 s56, s56, s59\n"
    "s_lshl_b32 s58, s56, 13\n"
    "buffer_load_dword v66, v54, s[44:47], s58 offen\n"
    "s_add_u32 s56, s56, 16\n"
    "s_sub_u32 s59, s56, 196\n"
    "s_cselect_b32 s56, s56, s59\n"
    "s_lshl_b32 s58, s56, 13\n"
    "s_waitcnt vmcnt(1)\n"
    "buffer_load_dword v67, v54, s[44:47], s58 offen\n"
    "s_add_u32 s56, s56, 16\n"
    "s_sub_u32 s59, s56, 196\n"
    "s_cselect_b32 s56, s56, s59\n"
    "s_lshl_b32 s58, s56, 13\n"
    "s_waitcnt vmcnt(1)\n"
    "buffer_load_dword v68, v54, s[44:47], s58 offen\n"
    "s_add_u32 s56, s56, 16\n"
    "s_sub_u32 s59, s56, 196\n"
    "s_cselect_b32 s56, s56, s59\n"
    "s_lshl_b32 s58, s56, 13\n"
    "s_waitcnt vmcnt(1)\n"
    "buffer_load_dword v69, v54, s[44:47], s58 offen\n"
    "s_add_u32 s56, s56, 16\n"
    "s_sub_u32 s59, s56, 196\n"
    "s_cselect_b32 s56, s56, s59\n"
    "s_lshl_b32 s58, s56, 13\n"
    "s_waitcnt vmcnt(1)\n"
    "buffer_load_dword v70, v54, s[44:47], s58 offen\n"
    "s_add_u32 s56, s56, 16\n"
    "s_sub_u32 s59, s56, 196\n"
    "s_cselect_b32 s56, s56, s59\n"
    "s_lshl_b32 s58, s56, 13\n"
    "s_waitcnt vmcnt(1)\n"
    "buffer_load_dword v71, v54, s[44:47], s58 offen\n"
    "s_add_u32 s56, s56, 16\n"
    ".Lsp_end%=:\n"
    "s_waitcnt vmcnt(0)\n"
    "s_endpgm\n"
    ".fill (2960 - (. - .Lstart%=)) / 4, 4, 0xbf800000\n"
    ".Lpage1_%=:\n"
    "; ---- set-up done\n"
    "s_waitcnt lgkmcnt(0)\n"
    "s_waitcnt vmcnt(11)\n"
    "s_barrier\n"
    "; ---- phase 1: K loop\n"
    "ds_read_b64_tr_b16 v[208:209], v20 offset:16384\n"
    "ds_read_b64_tr_b16 v[210:211], v20 offset:17408\n"
    "ds_read_b128 v[64:67], v14 offset:0\n"
    "ds_read_b64_tr_b16 v[216:217], v20 offset:24576\n"
    "ds_read_b64_tr_b16 v[218:219], v20 offset:25600\n"
    "ds_read_b128 v[68:71], v15 offset:0\n"
    "ds_read_b64_tr_b16 v[220:221], v21 offset:24576\n"
    "ds_read_b64_tr_b16 v[222:223], v21 offset:25600\n"
    "ds_read_b64_tr_b16 v[212:213], v21 offset:16384\n"
    "ds_read_b64_tr_b16 v[214:215], v21 offset:17408\n"
    "ds_read_b128 v[72:75], v14 offset:2048\n"
    "ds_read_b128 v[76:79], v15 offset:2048\n"
    "s_waitcnt lgkmcnt(9)\n"
    "v_mfma_f32_16x16x32_bf16 v[112:115], v[208:211], v[64:67], 0\n"
    "ds_read_b128 v[80:83], v14 offset:8192\n"
    "s_waitcnt lgkmcnt(7)\n"
    "v_mfma_f32_16x16x32_bf16 v[112:115], v[216:219], v[68:71], v[112:115]\n"
    "ds_read_b128 v[84:87], v15 offset:8192\n"
    "s_waitcnt lgkmcnt(6)\n"
    "v_mfma_f32_16x16x32_bf16 v[116:119], v[220:223], v[68:71], 0\n"
    "buffer_load_dwordx4 v4, s[36:39], s6 offen offset:2048 lds\n"
    "s_waitcnt lgkmcnt(4)\n"
    "v_mfma_f32_16x16x32_bf16 v[116:119], v[212:215], v[64:67], v[116:119]\n"
    "ds_read_b128 v[88:91], v14 offset:10240\n"
    "s_waitcnt lgkmcnt(4)\n"
    "v_mfma_f32_16x16x32_bf16 v[120:123], v[208:211], v[72:75], 0\n"
    "ds_read_b128 v[92:95], v15 offset:10240\n"
    "s_waitcnt lgkmcnt(4)\n"
    "v_mfma_f32_16x16x32_bf16 v[120:123], v[216:219], v[76:79], v[120:123]\n"
    "buffer_load_dwordx4 v5, s[36:39], s6 offen offset:3072 lds\n"
    "v_mfma_f32_16x16x32_bf16 v[124:127], v[220:223], v[76:79], 0\n"
    "s_add_u32 m0, s28, 81920\n"
    "v_mfma_f32_16x16x32_bf16 v[124:127], v[212:215], v[72:75], v[124:127]\n"
    "buffer_load_dwordx4 v6, s[40:43], s11 offen offset:0 lds\n"
    "s_waitcnt lgkmcnt(2)\n"
    "v_mfma_f32_16x16x32_bf16 v[144:147], v[208:211], v[80:83], 0\n"
    "ds_read_b128 v[64:67], v14 offset:4096\n"
    "v_mfma_f32_16x16x32_bf16 v[144:147], v[216:219], v[84:87], v[144:147]\n"
    "ds_read_b128 v[68:71], v15 offset:4096\n"
    "s_waitcnt lgkmcnt(2)\n"
    "v_mfma_f32_16x16x32_bf16 v[148:151], v[220:223], v[84:87], 0\n"
    "buffer_load_dwordx4 v7, s[40:43], s11 offen offset:1024 lds\n"
    "v_mfma_f32_16x16x32_bf16 v[148:151], v[212:215], v[80:83], v[148:151]\n"
    "ds_read_b128 v[72:75], v14 offset:6144\n"
    "v_mfma_f32_16x16x32_bf16 v[152:155], v[208:211], v[88:91], 0\n"
    "ds_read_b128 v[76:79], v15 offset:6144\n"
    "v_mfma_f32_16x16x32_bf16 v[152:155], v[216:219], v[92:95], v[152:155]\n"
    "buffer_load_dwordx4 v8, s[40:43], s11 offen offset:2048 lds\n"
    "v_mfma_f32_16x16x32_bf16 v[156:159], v[220:223], v[92:95], 0\n"
    "v_mfma_f32_16x16x32_bf16 v[156:159], v[212:215], v[88:91], v[156:159]\n"
    "buffer_load_dwordx4 v9, s[40:43], s11 offen offset:3072 lds\n"
    "s_waitcnt lgkmcnt(2)\n"
    "v_mfma_f32_16x16x32_bf16 v[128:131], v[208:211], v[64:67], 0\n"
    "ds_read_b128 v[80:83], v14 offset:12288\n"
    "s_add_u32 m0, s28, 98304\n"
    "v_mfma_f32_16x16x32_bf16 v[128:131], v[216:219], v[68:71], v[128:131]\n"
    "ds_read_b128 v[84:87], v15 offset:12288\n"
    "s_waitcnt lgkmcnt(2)\n"
    "v_mfma_f32_16x16x32_bf16 v[132:135], v[220:223], v[68:71], 0\n"
    "buffer_load_dwordx4 v2, s[36:39], s7 offen offset:0 lds\n"
    "v_mfma_f32_16x16x32_bf16 v[132:135], v[212:215], v[64:67], v[132:135]\n"
    "ds_read_b128 v[88:91], v14 offset:14336\n"
    "v_mfma_f32_16x16x32_bf16 v[136:139], v[208:211], v[72:75], 0\n"
    "ds_read_b128 v[92:95], v15 offset:14336\n"
    "v_mfma_f32_16x16x32_bf16 v[136:139], v[216:219], v[76:79], v[136:139]\n"
    "buffer_load_dwordx4 v3, s[36:39], s7 offen offset:1024 lds\n"
    "v_mfma_f32_16x16x32_bf16 v[140:143], v[220:223], v[76:79], 0\n"
    "v_mfma_f32_16x16x32_bf16 v[140:143], v[212:215], v[72:75], v[140:143]\n"
    "buffer_load_dwordx4 v4, s[36:39], s7 offen offset:2048 lds\n"
    "s_waitcnt lgkmcnt(0)\n"
    "v_mfma_f32_16x16x32_bf16 v[160:163], v[208:211], v[80:83], 0\n"
    "s_waitcnt vmcnt(11)\n"
    "s_barrier\n"
    "ds_read_b64_tr_b16 v[224:225], v20 offset:49152\n"
    "ds_read_b64_tr_b16 v[226:227], v20 offset:50176\n"
    "v_mfma_f32_16x16x32_bf16 v[160:163], v[216:219], v[84:87], v[160:163]\n"
    "ds_read_b128 v[64:67], v14 offset:32768\n"
    "ds_read_b64_tr_b16 v[232:233], v20 offset:57344\n"
    "v_mfma_f32_16x16x32_bf16 v[164:167], v[220:223], v[84:87], 0\n"
    "ds_read_b64_tr_b16 v[234:235], v20 offset:58368\n"
    "ds_read_b128 v[68:71], v15 offset:32768\n"
    "v_mfma_f32_16x16x32_bf16 v[164:167], v[212:215], v[80:83], v[164:167]\n"
    "ds_read_b64_tr_b16 v[236:237], v21 offset:57344\n"
    "ds_read_b64_tr_b16 v[238:239], v21 offset:58368\n"
    "v_mfma_f32_16x16x32_bf16 v[168:171], v[208:211], v[88:91], 0\n"
    "ds_read_b64_tr_b16 v[228:229], v21 offset:49152\n"
    "ds_read_b64_tr_b16 v[230:231], v21 offset:50176\n"
    "v_mfma_f32_16x16x32_bf16 v[168:171], v[216:219], v[92:95], v[168:171]\n"
    "ds_read_b128 v[72:75], v14 offset:34816\n"
    "ds_read_b128 v[76:79], v15 offset:34816\n"
    "v_mfma_f32_16x16x32_bf16 v[172:175], v[220:223], v[92:95], 0\n"
    "v_mfma_f32_16x16x32_bf16 v[172:175], v[212:215], v[88:91], v[172:175]\n"
    "buffer_load_dwordx4 v5, s[36:39], s7 offen offset:3072 lds\n"
    "s_waitcnt lgkmcnt(9)\n"
    "v_mfma_f32_16x16x32_bf16 v[112:115], v[224:227], v[64:67], v[112:115]\n"
    "ds_read_b128 v[80:83], v14 offset:40960\n"
    "s_add_u32 m0, s28, 114688\n"
    "s_waitcnt lgkmcnt(7)\n"
    "v_mfma_f32_16x16x32_bf16 v[112:115], v[232:235], v[68:71], v[112:115]\n"
    "ds_read_b128 v[84:87], v15 offset:40960\n"
    "s_waitcnt lgkmcnt(6)\n"
    "v_mfma_f32_16x16x32_bf16 v[116:119], v[236:239], v[68:71], v[116:119]\n"
    "buffer_load_dwordx4 v6, s[40:43], s12 offen offset:0 lds\n"
    "s_waitcnt lgkmcnt(4)\n"
    "v_mfma_f32_16x16x32_bf16 v[116:119], v[228:231], v[64:67], v[116:119]\n"
    "ds_read_b128 v[88:91], v14 offset:43008\n"
    "s_waitcnt lgkmcnt(4)\n"
    "v_mfma_f32_16x16x32_bf16 v[120:123], v[224:227], v[72:75], v[120:123]\n"
    "ds_read_b128 v[92:95], v15 offset:43008\n"
    "s_waitcnt lgkmcnt(4)\n"
    "v_mfma_f32_16x16x32_bf16 v[120:123], v[232:235], v[76:79], v[120:123]\n"
    "buffer_load_dwordx4 v7, s[40:43], s12 offen offset:1024 lds\n"
    "v_mfma_f32_16x16x32_bf16 v[124:127], v[236:239], v[76:79], v[124:127]\n"
    "v_mfma_f32_16x16x32_bf16 v[124:127], v[228:231], v[72:75], v[124:127]\n"
    "buffer_load_dwordx4 v8, s[40:43], s12 offen offset:2048 lds\n"
    "s_waitcnt lgkmcnt(2)\n"
    "v_mfma_f32_16x16x32_bf16 v[144:147], v[224:227], v[80:83], v[144:147]\n"
    "ds_read_b128 v[64:67], v14 offset:36864\n"
    "v_mfma_f32_16x16x32_bf16 v[144:147], v[232:235], v[84:87], v[144:147]\n"
    "ds_read_b128 v[68:71], v15 offset:36864\n"
    "s_waitcnt lgkmcnt(2)\n"
    "v_mfma_f32_16x16x32_bf16 v[148:151], v[236:239], v[84:87], v[148:151]\n"
    "buffer_load_dwordx4 v9, s[40:43], s12 offen offset:3072 lds\n"
    "v_mfma_f32_16x16x32_bf16 v[148:151], v[228:231], v[80:83], v[148:151]\n"
    "ds_read_b128 v[72:75], v14 offset:38912\n"
    "s_add_u32 m0, s28, 131072\n"
    "v_mfma_f32_16x16x32_bf16 v[152:155], v[224:227], v[88:91], v[152:155]\n"
    "ds_read_b128 v[76:79], v15 offset:38912\n"
    "v_mfma_f32_16x16x32_bf16 v[152:155], v[232:235], v[92:95], v[152:155]\n"
    "buffer_load_dwordx4 v2, s[36:39], s8 offen offset:0 lds\n"
    "v_mfma_f32_16x16x32_bf16 v[156:159], v[236:239], v[92:95], v[156:159]\n"
    "v_mfma_f32_16x16x32_bf16 v[156:159], v[228:231], v[88:91], v[156:159]\n"
    "buffer_load_dwordx4 v3, s[36:39], s8 offen offset:1024 lds\n"
    "s_waitcnt lgkmcnt(2)\n"
    "v_mfma_f32_16x16x32_bf16 v[128:131], v[224:227], v[64:67], v[128:131]\n"
    "ds_read_b128 v[80:83], v14 offset:45056\n"
    "v_mfma_f32_16x16x32_bf16 v[128:131], v[232:235], v[68:71], v[128:131]\n"
    "ds_read_b128 v[84:87], v15 offset:45056\n"
    "s_waitcnt lgkmcnt(2)\n"
    "v_mfma_f32_16x16x32_bf16 v[132:135], v[236:239], v[68:71], v[132:135]\n"
    "buffer_load_dwordx4 v4, s[36:39], s8 offen offset:2048 lds\n"
    "v_mfma_f32_16x16x32_bf16 v[132:135], v[228:231], v[64:67], v[132:135]\n"
    "ds_read_b128 v[88:91], v14 offset:47104\n"
    "v_mfma_f32_16x16x32_bf16 v[136:139], v[224:227], v[72:75], v[136:139]\n"
    "ds_read_b128 v[92:95], v15 offset:47104\n"
    "v_mfma_f32_16x16x32_bf16 v[136:139], v[232:235], v[76:79], v[136:139]\n"
    "buffer_load_dwordx4 v5, s[36:39], s8 offen offset:3072 lds\n"
    "v_mfma_f32_16x16x32_bf16 v[140:143], v[236:239], v[76:79], v[140:143]\n"
    "s_add_u32 m0, s28, 147456\n"
    "v_mfma_f32_16x16x32_bf16 v[140:143], v[228:231], v[72:75], v[140:143]\n"
    "buffer_load_dwordx4 v6, s[40:43], s13 offen offset:0 lds\n"
    "s_waitcnt lgkmcnt(0)\n"
    "v_mfma_f32_16x16x32_bf16 v[160:163], v[224:227], v[80:83], v[160:163]\n"
    "s_waitcnt vmcnt(13)\n"
    "s_barrier\n"
    "ds_read_b64_tr_b16 v[240:241], v22 offset:16384\n"
    "ds_read_b64_tr_b16 v[242:243], v22 offset:17408\n"
    "v_mfma_f32_16x16x32_bf16 v[160:163], v[232:235], v[84:87], v[160:163]\n"
    "ds_read_b128 v[64:67], v16 offset:0\n"
    "ds_read_b64_tr_b16 v[248:249], v22 offset:24576\n"
    "v_mfma_f32_16x16x32_bf16 v[164:167], v[236:239], v[84:87], v[164:167]\n"
    "ds_read_b64_tr_b16 v[250:251], v22 offset:25600\n"
    "ds_read_b128 v[68:71], v17 offset:0\n"
    "v_mfma_f32_16x16x32_bf16 v[164:167], v[228:231], v[80:83], v[164:167]\n"
    "ds_read_b64_tr_b16 v[252:253], v23 offset:24576\n"
    "ds_read_b64_tr_b16 v[254:255], v23 offset:25600\n"
    "v_mfma_f32_16x16x32_bf16 v[168:171], v[224:227], v[88:91], v[168:171]\n"
    "ds_read_b64_tr_b16 v[244:245], v23 offset:16384\n"
    "ds_read_b64_tr_b16 v[246:247], v23 offset:17408\n"
    "v_mfma_f32_16x16x32_bf16 v[168:171], v[232:235], v[92:95], v[168:171]\n"
    "ds_read_b128 v[72:75], v16 offset:2048\n"
    "ds_read_b128 v[76:79], v17 offset:2048\n"
    "v_mfma_f32_16x16x32_bf16 v[172:175], v[236:239], v[92:95], v[172:175]\n"
    "v_mfma_f32_16x16x32_bf16 v[172:175], v[228:231], v[88:91], v[172:175]\n"
    "buffer_load_dwordx4 v7, s[40:43], s13 offen offset:1024 lds\n"
    "v_add_co_u32 v56, vcc, 0x7f4a7c15, v38\n"
    "v_mov_b32 v40, 0x9e3779b9\n"
    "v_mov_b32 v58, v56\n"
    "v_addc_co_u32 v57, vcc, v40, v39, vcc\n"
    "v_mov_b32 v59, v57\n"
    "s_waitcnt lgkmcnt(9)\n"
    "v_mfma_f32_16x16x32_bf16 v[112:115], v[240:243], v[64:67], v[112:115]\n"
    "ds_read_b128 v[80:83], v16 offset:8192\n"
    "s_waitcnt lgkmcnt(7)\n"
    "v_mfma_f32_16x16x32_bf16 v[112:115], v[248:251], v[68:71], v[112:115]\n"
    "ds_read_b128 v[84:87], v17 offset:8192\n"
    "s_waitcnt lgkmcnt(6)\n"
    "v_mfma_f32_16x16x32_bf16 v[116:119], v[252:255], v[68:71], v[116:119]\n"
    "buffer_load_dwordx4 v8, s[40:43], s13 offen offset:2048 lds\n"
    "s_waitcnt lgkmcnt(4)\n"
    "v_mfma_f32_16x16x32_bf16 v[116:119], v[244:247], v[64:67], v[116:119]\n"
    "ds_read_b128 v[88:91], v16 offset:10240\n"
    "s_waitcnt lgkmcnt(4)\n"
    "v_mfma_f32_16x16x32_bf16 v[120:123], v[240:243], v[72:75], v[120:123]\n"
    "ds_read_b128 v[92:95], v17 offset:10240\n"
    "s_waitcnt lgkmcnt(4)\n"
    "v_mfma_f32_16x16x32_bf16 v[120:123], v[248:251], v[76:79], v[120:123]\n"
    "buffer_load_dwordx4 v9, s[40:43], s13 offen offset:3072 lds\n"
    "v_mfma_f32_16x16x32_bf16 v[124:127], v[252:255], v[76:79], v[124:127]\n"
    "s_add_u32 m0, s28, 0\n"
    "v_mfma_f32_16x16x32_bf16 v[124:127], v[244:247], v[72:75], v[124:127]\n"
    "buffer_load_dwordx4 v2, s[36:39], s9 offen offset:0 lds\n"
    "s_waitcnt lgkmcnt(2)\n"
    "v_mfma_f32_16x16x32_bf16 v[144:147], v[240:243], v[80:83], v[144:147]\n"
    "ds_read_b128 v[64:67], v16 offset:4096\n"
    "v_mfma_f32_16x16x32_bf16 v[144:147], v[248:251], v[84:87], v[144:147]\n"
    "ds_read_b128 v[68:71], v17 offset:4096\n"
    "s_waitcnt lgkmcnt(2)\n"
    "v_mfma_f32_16x16x32_bf16 v[148:151], v[252:255], v[84:87], v[148:151]\n"
    "buffer_load_dwordx4 v3, s[36:39], s9 offen offset:1024 lds\n"
    "v_mfma_f32_16x16x32_bf16 v[148:151], v[244:247], v[80:83], v[148:151]\n"
    "ds_read_b128 v[72:75], v16 offset:6144\n"
    "v_mfma_f32_16x16x32_bf16 v[152:155], v[240:243], v[88:91], v[152:155]\n"
    "ds_read_b128 v[76:79], v17 offset:6144\n"
    "v_mfma_f32_16x16x32_bf16 v[152:155], v[248:251], v[92:95], v[152:155]\n"
    "buffer_load_dwordx4 v4, s[36:39], s9 offen offset:2048 lds\n"
    "v_mfma_f32_16x16x32_bf16 v[156:159], v[252:255], v[92:95], v[156:159]\n"
    "v_mfma_f32_16x16x32_bf16 v[156:159], v[244:247], v[88:91], v[156:159]\n"
    "buffer_load_dwordx4 v5, s[36:39], s9 offen offset:3072 lds\n"
    "s_waitcnt lgkmcnt(2)\n"
    "v_mfma_f32_16x16x32_bf16 v[128:131], v[240:243], v[64:67], v[128:131]\n"
    "ds_read_b128 v[80:83], v16 offset:12288\n"
    "s_add_u32 m0, s28, 16384\n"
    "v_mfma_f32_16x16x32_bf16 v[128:131], v[248:251], v[68:71], v[128:131]\n"
    "ds_read_b128 v[84:87], v17 offset:12288\n"
    "s_waitcnt lgkmcnt(2)\n"
    "v_mfma_f32_16x16x32_bf16 v[132:135], v[252:255], v[68:71], v[132:135]\n"
    "buffer_load_dwordx4 v6, s[40:43], s14 offen offset:0 lds\n"
    "v_mfma_f32_16x16x32_bf16 v[132:135], v[244:247], v[64:67], v[132:135]\n"
    "ds_read_b128 v[88:91], v16 offset:14336\n"
    "v_mfma_f32_16x16x32_bf16 v[136:139], v[240:243], v[72:75], v[136:139]\n"
    "ds_read_b128 v[92:95], v17 offset:14336\n"
    "v_mfma_f32_16x16x32_bf16 v[136:139], v[248:251], v[76:79], v[136:139]\n"
    "buffer_load_dwordx4 v7, s[40:43], s14 offen offset:1024 lds\n"
    "v_mfma_f32_16x16x32_bf16 v[140:143], v[252:255], v[76:79], v[140:143]\n"
    "v_mfma_f32_16x16x32_bf16 v[140:143], v[244:247], v[72:75], v[140:143]\n"
    "buffer_load_dwordx4 v8, s[40:43], s14 offen offset:2048 lds\n"
    "s_waitcnt lgkmcnt(0)\n"
    "v_mfma_f32_16x16x32_bf16 v[160:163], v[240:243], v[80:83], v[160:163]\n"
    "s_waitcnt vmcnt(15)\n"
    "s_barrier\n"
    "ds_read_b64_tr_b16 v[208:209], v22 offset:49152\n"
    "ds_read_b64_tr_b16 v[210:211], v22 offset:50176\n"
    "v_mfma_f32_16x16x32_bf16 v[160:163], v[248:251], v[84:87], v[160:163]\n"
    "ds_read_b128 v[64:67], v16 offset:32768\n"
    "ds_read_b64_tr_b16 v[216:217], v22 offset:57344\n"
    "v_mfma_f32_16x16x32_bf16 v[164:167], v[252:255], v[84:87], v[164:167]\n"
    "ds_read_b64_tr_b16 v[218:219], v22 offset:58368\n"
    "ds_read_b128 v[68:71], v17 offset:32768\n"
    "v_mfma_f32_16x16x32_bf16 v[164:167], v[244:247], v[80:83], v[164:167]\n"
    "ds_read_b64_tr_b16 v[220:221], v23 offset:57344\n"
    "ds_read_b64_tr_b16 v[222:223], v23 offset:58368\n"
    "v_mfma_f32_16x16x32_bf16 v[168:171], v[240:243], v[88:91], v[168:171]\n"
    "ds_read_b64_tr_b16 v[212:213], v23 offset:49152\n"
    "ds_read_b64_tr_b16 v[214:215], v23 offset:50176\n"
    "v_mfma_f32_16x16x32_bf16 v[168:171], v[248:251], v[92:95], v[168:171]\n"
    "ds_read_b128 v[72:75], v16 offset:34816\n"
    "ds_read_b128 v[76:79], v17 offset:34816\n"
    "v_mfma_f32_16x16x32_bf16 v[172:175], v[252:255], v[92:95], v[172:175]\n"
    "v_mfma_f32_16x16x32_bf16 v[172:175], v[244:247], v[88:91], v[172:175]\n"
    "buffer_load_dwordx4 v9, s[40:43], s14 offen offset:3072 lds\n"
    "s_waitcnt lgkmcnt(9)\n"
    "v_mfma_f32_16x16x32_bf16 v[112:115], v[208:211], v[64:67], v[112:115]\n"
    "ds_read_b128 v[80:83], v16 offset:40960\n"
    "s_add_u32 m0, s28, 32768\n"
    "s_waitcnt lgkmcnt(7)\n"
    "v_mfma_f32_16x16x32_bf16 v[112:115], v[216:219], v[68:71], v[112:115]\n"
    "ds_read_b128 v[84:87], v17 offset:40960\n"
    "s_waitcnt lgkmcnt(6)\n"
    "v_mfma_f32_16x16x32_bf16 v[116:119], v[220:223], v[68:71], v[116:119]\n"
    "buffer_load_dwordx4 v2, s[36:39], s10 offen offset:0 lds\n"
    "s_waitcnt lgkmcnt(4)\n"
    "v_mfma_f32_16x16x32_bf16 v[116:119], v[212:215], v[64:67], v[116:119]\n"
    "ds_read_b128 v[88:91], v16 offset:43008\n"
    "s_waitcnt lgkmcnt(4)\n"
    "v_mfma_f32_16x16x32_bf16 v[120:123], v[208:211], v[72:75], v[120:123]\n"
    "ds_read_b128 v[92:95], v17 offset:43008\n"
    "s_waitcnt lgkmcnt(4)\n"
    "v_mfma_f32_16x16x32_bf16 v[120:123], v[216:219], v[76:79], v[120:123]\n"
    "buffer_load_dwordx4 v3, s[36:39], s10 offen offset:1024 lds\n"
    "v_mfma_f32_16x16x32_bf16 v[124:127], v[220:223], v[76:79], v[124:127]\n"
    "v_mfma_f32_16x16x32_bf16 v[124:127], v[212:215], v[72:75], v[124:127]\n"
    "buffer_load_dwordx4 v4, s[36:39], s10 offen offset:2048 lds\n"
    "s_waitcnt lgkmcnt(2)\n"
    "v_mfma_f32_16x16x32_bf16 v[144:147], v[208:211], v[80:83], v[144:147]\n"
    "ds_read_b128 v[64:67], v16 offset:36864\n"
    "v_mfma_f32_16x16x32_bf16 v[144:147], v[216:219], v[84:87], v[144:147]\n"
    "ds_read_b128 v[68:71], v17 offset:36864\n"
    "s_waitcnt lgkmcnt(2)\n"
    "v_mfma_f32_16x16x32_bf16 v[148:151], v[220:223], v[84:87], v[148:151]\n"
    "buffer_load_dwordx4 v5, s[36:39], s10 offen offset:3072 lds\n"
    "v_mfma_f32_16x16x32_bf16 v[148:151], v[212:215], v[80:83], v[148:151]\n"
    "ds_read_b128 v[72:75], v16 offset:38912\n"
    "s_add_u32 m0, s28, 49152\n"
    "v_mfma_f32_16x16x32_bf16 v[152:155], v[208:211], v[88:91], v[152:155]\n"
    "ds_read_b128 v[76:79], v17 offset:38912\n"
    "v_mfma_f32_16x16x32_bf16 v[152:155], v[216:219], v[92:95], v[152:155]\n"
    "buffer_load_dwordx4 v6, s[40:43], s15 offen offset:0 lds\n"
    "v_mfma_f32_16x16x32_bf16 v[156:159], v[220:223], v[92:95], v[156:159]\n"
    "v_mfma_f32_16x16x32_bf16 v[156:159], v[212:215], v[88:91], v[156:159]\n"
    "buffer_load_dwordx4 v7, s[40:43], s15 offen offset:1024 lds\n"
    "s_waitcnt lgkmcnt(2)\n"
    "v_mfma_f32_16x16x32_bf16 v[128:131], v[208:211], v[64:67], v[128:131]\n"
    "ds_read_b128 v[80:83], v16 offset:45056\n"
    "v_mfma_f32_16x16x32_bf16 v[128:131], v[216:219], v[68:71], v[128:131]\n"
    "ds_read_b128 v[84:87], v17 offset:45056\n"
    "s_waitcnt lgkmcnt(2)\n"
    "v_mfma_f32_16x16x32_bf16 v[132:135], v[220:223], v[68:71], v[132:135]\n"
    "buffer_load_dwordx4 v8, s[40:43], s15 offen offset:2048 lds\n"
    "v_mfma_f32_16x16x32_bf16 v[132:135], v[212:215], v[64:67], v[132:135]\n"
    "ds_read_b128 v[88:91], v16 offset:47104\n"
    "v_mfma_f32_16x16x32_bf16 v[136:139], v[208:211], v[72:75], v[136:139]\n"
    "ds_read_b128 v[92:95], v17 offset:47104\n"
    "v_mfma_f32_16x16x32_bf16 v[136:139], v[216:219], v[76:79], v[136:139]\n"
    "buffer_load_dwordx4 v9, s[40:43], s15 offen offset:3072 lds\n"
    "v_mfma_f32_16x16x32_bf16 v[140:143], v[220:223], v[76:79], v[140:143]\n"
    "s_add_u32 m0, s29, 65536\n"
    "v_mfma_f32_16x16x32_bf16 v[140:143], v[212:215], v[72:75], v[140:143]\n"
    "buffer_load_dwordx4 v10, s[44:47], s66 offen offset:0 lds\n"
    "s_waitcnt lgkmcnt(0)\n"
    "v_mfma_f32_16x16x32_bf16 v[160:163], v[208:211], v[80:83], v[160:163]\n"
    "s_waitcnt vmcnt(17)\n"
    "s_barrier\n"
    "ds_read_b64_tr_b16 v[224:225], v24 offset:16384\n"
    "ds_read_b64_tr_b16 v[226:227], v24 offset:17408\n"
    "v_mfma_f32_16x16x32_bf16 v[160:163], v[216:219], v[84:87], v[160:163]\n"
    "ds_read_b128 v[64:67], v18 offset:0\n"
    "ds_read_b64_tr_b16 v[232:233], v24 offset:24576\n"
    "v_mfma_f32_16x16x32_bf16 v[164:167], v[220:223], v[84:87], v[164:167]\n"
    "ds_read_b64_tr_b16 v[234:235], v24 offset:25600\n"
    "ds_read_b128 v[68:71], v19 offset:0\n"
    "v_mfma_f32_16x16x32_bf16 v[164:167], v[212:215], v[80:83], v[164:167]\n"
    "ds_read_b64_tr_b16 v[236:237], v25 offset:24576\n"
    "ds_read_b64_tr_b16 v[238:239], v25 offset:25600\n"
    "v_mfma_f32_16x16x32_bf16 v[168:171], v[208:211], v[88:91], v[168:171]\n"
    "ds_read_b64_tr_b16 v[228:229], v25 offset:16384\n"
    "ds_read_b64_tr_b16 v[230:231], v25 offset:17408\n"
    "v_mfma_f32_16x16x32_bf16 v[168:171], v[216:219], v[92:95], v[168:171]\n"
    "ds_read_b128 v[72:75], v18 offset:2048\n"
    "ds_read_b128 v[76:79], v19 offset:2048\n"
    "v_mfma_f32_16x16x32_bf16 v[172:175], v[220:223], v[92:95], v[172:175]\n"
    "v_mfma_f32_16x16x32_bf16 v[172:175], v[212:215], v[88:91], v[172:175]\n"
    "buffer_load_dwordx4 v11, s[44:47], s66 offen offset:1024 lds\n"
    "s_waitcnt lgkmcnt(9)\n"
    "v_mfma_f32_16x16x32_bf16 v[112:115], v[224:227], v[64:67], v[112:115]\n"
    "ds_read_b128 v[80:83], v18 offset:8192\n"
    "s_waitcnt lgkmcnt(7)\n"
    "v_mfma_f32_16x16x32_bf16 v[112:115], v[232:235], v[68:71], v[112:115]\n"
    "ds_read_b128 v[84:87], v19 offset:8192\n"
    "s_waitcnt lgkmcnt(6)\n"
    "v_mfma_f32_16x16x32_bf16 v[116:119], v[236:239], v[68:71], v[116:119]\n"
    "buffer_load_dwordx4 v12, s[44:47], s66 offen offset:2048 lds\n"
    "s_waitcnt lgkmcnt(4)\n"
    "v_mfma_f32_16x16x32_bf16 v[116:119], v[228:231], v[64:67], v[116:119]\n"
    "ds_read_b128 v[88:91], v18 offset:10240\n"
    "s_waitcnt lgkmcnt(4)\n"
    "v_mfma_f32_16x16x32_bf16 v[120:123], v[224:227], v[72:75], v[120:123]\n"
    "ds_read_b128 v[92:95], v19 offset:10240\n"
    "s_waitcnt lgkmcnt(4)\n"
    "v_mfma_f32_16x16x32_bf16 v[120:123], v[232:235], v[76:79], v[120:123]\n"
    "buffer_load_dwordx4 v13, s[44:47], s66 offen offset:3072 lds\n"
    "v_mfma_f32_16x16x32_bf16 v[124:127], v[236:239], v[76:79], v[124:127]\n"
    "s_add_u32 m0, s29, 69632\n"
    "v_mfma_f32_16x16x32_bf16 v[124:127], v[228:231], v[72:75], v[124:127]\n"
    "buffer_load_dwordx4 v10, s[44:47], s67 offen offset:0 lds\n"
    "s_waitcnt lgkmcnt(2)\n"
    "v_mfma_f32_16x16x32_bf16 v[144:147], v[224:227], v[80:83], v[144:147]\n"
    "ds_read_b128 v[64:67], v18 offset:4096\n"
    "v_mfma_f32_16x16x32_bf16 v[144:147], v[232:235], v[84:87], v[144:147]\n"
    "ds_read_b128 v[68:71], v19 offset:4096\n"
    "s_waitcnt lgkmcnt(2)\n"
    "v_mfma_f32_16x16x32_bf16 v[148:151], v[236:239], v[84:87], v[148:151]\n"
    "buffer_load_dwordx4 v11, s[44:47], s67 offen offset:1024 lds\n"
    "v_mfma_f32_16x16x32_bf16 v[148:151], v[228:231], v[80:83], v[148:151]\n"
    "ds_read_b128 v[72:75], v18 offset:6144\n"
    "v_mfma_f32_16x16x32_bf16 v[152:155], v[224:227], v[88:91], v[152:155]\n"
    "ds_read_b128 v[76:79], v19 offset:6144\n"
    "v_mfma_f32_16x16x32_bf16 v[152:155], v[232:235], v[92:95], v[152:155]\n"
    "buffer_load_dwordx4 v12, s[44:47], s67 offen offset:2048 lds\n"
    "v_mfma_f32_16x16x32_bf16 v[156:159], v[236:239], v[92:95], v[156:159]\n"
    "v_mfma_f32_16x16x32_bf16 v[156:159], v[228:231], v[88:91], v[156:159]\n"
    "buffer_load_dwordx4 v13, s[44:47], s67 offen offset:3072 lds\n"
    "s_waitcnt lgkmcnt(0)\n"
    "v_mfma_f32_16x16x32_bf16 v[128:131], v[224:227], v[64:67], v[128:131]\n"
    "s_waitcnt vmcnt(16)\n"
    "s_barrier\n"
    "ds_read_b128 v[80:83], v18 offset:12288\n"
    "ds_read_b128 v[84:87], v19 offset:12288\n"
    "v_mfma_f32_16x16x32_bf16 v[128:131], v[232:235], v[68:71], v[128:131]\n"
    "ds_read_b128 v[88:91], v18 offset:14336\n"
    "ds_read_b128 v[92:95], v19 offset:14336\n"
    "v_mfma_f32_16x16x32_bf16 v[132:135], v[236:239], v[68:71], v[132:135]\n"
    "ds_read_b64_tr_b16 v[240:241], v20 offset:16384\n"
    "ds_read_b64_tr_b16 v[242:243], v20 offset:17408\n"
    "v_mfma_f32_16x16x32_bf16 v[132:135], v[228:231], v[64:67], v[132:135]\n"
    "ds_read_b64_tr_b16 v[248:249], v20 offset:24576\n"
    "ds_read_b64_tr_b16 v[250:251], v20 offset:25600\n"
    "v_mfma_f32_16x16x32_bf16 v[136:139], v[224:227], v[72:75], v[136:139]\n"
    "ds_read_b64_tr_b16 v[252:253], v21 offset:24576\n"
    "ds_read_b64_tr_b16 v[254:255], v21 offset:25600\n"
    "v_mfma_f32_16x16x32_bf16 v[136:139], v[232:235], v[76:79], v[136:139]\n"
    "ds_read_b64_tr_b16 v[244:245], v21 offset:16384\n"
    "ds_read_b64_tr_b16 v[246:247], v21 offset:17408\n"
    "v_mfma_f32_16x16x32_bf16 v[140:143], v[236:239], v[76:79], v[140:143]\n"
    "s_add_u32 m0, s29, 98304\n"
    "v_mfma_f32_16x16x32_bf16 v[140:143], v[228:231], v[72:75], v[140:143]\n"
    "buffer_load_dwordx4 v10, s[44:47], s68 offen offset:0 lds\n"
    "s_waitcnt lgkmcnt(0)\n"
    "v_mfma_f32_16x16x32_bf16 v[160:163], v[224:227], v[80:83], v[160:163]\n"
    "s_waitcnt vmcnt(9)\n"
    "s_barrier\n"
    "ds_read_b128 v[64:67], v14 offset:0\n"
    "ds_read_b128 v[68:71], v15 offset:0\n"
    "v_mfma_f32_16x16x32_bf16 v[160:163], v[232:235], v[84:87], v[160:163]\n"
    "ds_read_b64_tr_b16 v[208:209], v20 offset:49152\n"
    "ds_read_b64_tr_b16 v[210:211], v20 offset:50176\n"
    "v_mfma_f32_16x16x32_bf16 v[164:167], v[236:239], v[84:87], v[164:167]\n"
    "ds_read_b128 v[72:75], v14 offset:32768\n"
    "ds_read_b64_tr_b16 v[216:217], v20 offset:57344\n"
    "v_mfma_f32_16x16x32_bf16 v[164:167], v[228:231], v[80:83], v[164:167]\n"
    "ds_read_b64_tr_b16 v[218:219], v20 offset:58368\n"
    "ds_read_b128 v[76:79], v15 offset:32768\n"
    "v_mfma_f32_16x16x32_bf16 v[168:171], v[224:227], v[88:91], v[168:171]\n"
    "ds_read_b64_tr_b16 v[220:221], v21 offset:57344\n"
    "ds_read_b64_tr_b16 v[222:223], v21 offset:58368\n"
    "v_mfma_f32_16x16x32_bf16 v[168:171], v[232:235], v[92:95], v[168:171]\n"
    "ds_read_b64_tr_b16 v[212:213], v21 offset:49152\n"
    "ds_read_b64_tr_b16 v[214:215], v21 offset:50176\n"
    "v_mfma_f32_16x16x32_bf16 v[172:175], v[236:239], v[92:95], v[172:175]\n"
    "v_mfma_f32_16x16x32_bf16 v[172:175], v[228:231], v[88:91], v[172:175]\n"
    "buffer_load_dwordx4 v11, s[44:47], s68 offen offset:1024 lds\n"
    "s_waitcnt lgkmcnt(11)\n"
    "v_mfma_f32_16x16x32_bf16 v[112:115], v[240:243], v[64:67], v[112:115]\n"
    "ds_read_b128 v[80:83], v14 offset:2048\n"
    "s_waitcnt lgkmcnt(11)\n"
    "v_mfma_f32_16x16x32_bf16 v[112:115], v[248:251], v[68:71], v[112:115]\n"
    "ds_read_b128 v[84:87], v15 offset:2048\n"
    "s_waitcnt lgkmcnt(9)\n"
    "v_mfma_f32_16x16x32_bf16 v[112:115], v[208:211], v[72:75], v[112:115]\n"
    "buffer_load_dwordx4 v12, s[44:47], s68 offen offset:2048 lds\n"
    "s_waitcnt lgkmcnt(6)\n"
    "v_mfma_f32_16x16x32_bf16 v[112:115], v[216:219], v[76:79], v[112:115]\n"
    "ds_read_b128 v[88:91], v14 offset:34816\n"
    "s_waitcnt lgkmcnt(5)\n"
    "v_mfma_f32_16x16x32_bf16 v[116:119], v[220:223], v[76:79], v[116:119]\n"
    "ds_read_b128 v[92:95], v15 offset:34816\n"
    "s_waitcnt lgkmcnt(4)\n"
    "v_mfma_f32_16x16x32_bf16 v[116:119], v[212:215], v[72:75], v[116:119]\n"
    "buffer_load_dwordx4 v13, s[44:47], s68 offen offset:3072 lds\n"
    "v_mfma_f32_16x16x32_bf16 v[116:119], v[252:255], v[68:71], v[116:119]\n"
    "s_add_u32 m0, s29, 102400\n"
    "v_mfma_f32_16x16x32_bf16 v[116:119], v[244:247], v[64:67], v[116:119]\n"
    "buffer_load_dwordx4 v10, s[44:47], s69 offen offset:0 lds\n"
    "s_waitcnt lgkmcnt(0)\n"
    "v_mfma_f32_16x16x32_bf16 v[120:123], v[240:243], v[80:83], v[120:123]\n"
    "ds_read_b128 v[64:67], v14 offset:8192\n"
    "v_mfma_f32_16x16x32_bf16 v[120:123], v[248:251], v[84:87], v[120:123]\n"
    "ds_read_b128 v[68:71], v15 offset:8192\n"
    "v_mfma_f32_16x16x32_bf16 v[120:123], v[208:211], v[88:91], v[120:123]\n"
    "ds_read_b128 v[72:75], v14 offset:40960\n"
    "v_maximum3_f32 v112, v112, 0, 0\n"
    "v_maximum3_f32 v113, v113, 0, 0\n"
    "v_mfma_f32_16x16x32_bf16 v[120:123], v[216:219], v[92:95], v[120:123]\n"
    "ds_read_b128 v[76:79], v15 offset:40960\n"
    "v_cvt_pk_bf16_f32 v176, v112, v113\n"
    "v_maximum3_f32 v114, v114, 0, 0\n"
    "v_mfma_f32_16x16x32_bf16 v[124:127], v[220:223], v[92:95], v[124:127]\n"
    "v_maximum3_f32 v115, v115, 0, 0\n"
    "v_cvt_pk_bf16_f32 v177, v114, v115\n"
    "v_maximum3_f32 v116, v116, 0, 0\n"
    "v_mfma_f32_16x16x32_bf16 v[124:127], v[212:215], v[88:91], v[124:127]\n"
    "v_maximum3_f32 v117, v117, 0, 0\n"
    "v_cvt_pk_bf16_f32 v178, v116, v117\n"
    "v_maximum3_f32 v118, v118, 0, 0\n"
    "v_mfma_f32_16x16x32_bf16 v[124:127], v[252:255], v[84:87], v[124:127]\n"
    "v_maximum3_f32 v119, v119, 0, 0\n"
    "v_cvt_pk_bf16_f32 v179, v118, v119\n"
    "buffer_store_dwordx4 v[176:179], v42, s[48:51], s66 offen offset:0 sc1\n"
    "v_mfma_f32_16x16x32_bf16 v[124:127], v[244:247], v[80:83], v[124:127]\n"
    "buffer_load_dwordx4 v11, s[44:47], s69 offen offset:1024 lds\n"
    "s_waitcnt lgkmcnt(0)\n"
    "v_mfma_f32_16x16x32_bf16 v[144:147], v[240:243], v[64:67], v[144:147]\n"
    "ds_read_b128 v[80:83], v14 offset:10240\n"
    "v_mfma_f32_16x16x32_bf16 v[144:147], v[248:251], v[68:71], v[144:147]\n"
    "ds_read_b128 v[84:87], v15 offset:10240\n"
    "v_mfma_f32_16x16x32_bf16 v[144:147], v[208:211], v[72:75], v[144:147]\n"
    "ds_read_b128 v[88:91], v14 offset:43008\n"
    "v_maximum3_f32 v120, v120, 0, 0\n"
    "v_maximum3_f32 v121, v121, 0, 0\n"
    "v_mfma_f32_16x16x32_bf16 v[144:147], v[216:219], v[76:79], v[144:147]\n"
    "ds_read_b128 v[92:95], v15 offset:43008\n"
    "v_cvt_pk_bf16_f32 v180, v120, v121\n"
    "v_maximum3_f32 v122, v122, 0, 0\n"
    "v_mfma_f32_16x16x32_bf16 v[148:151], v[220:223], v[76:79], v[148:151]\n"
    "v_maximum3_f32 v123, v123, 0, 0\n"
    "v_cvt_pk_bf16_f32 v181, v122, v123\n"
    "v_maximum3_f32 v124, v124, 0, 0\n"
    "v_mfma_f32_16x16x32_bf16 v[148:151], v[212:215], v[72:75], v[148:151]\n"
    "v_maximum3_f32 v125, v125, 0, 0\n"
    "v_cvt_pk_bf16_f32 v182, v124, v125\n"
    "v_maximum3_f32 v126, v126, 0, 0\n"
    "v_mfma_f32_16x16x32_bf16 v[148:151], v[252:255], v[68:71], v[148:151]\n"
    "v_maximum3_f32 v127, v127, 0, 0\n"
    "v_cvt_pk_bf16_f32 v183, v126, v127\n"
    "buffer_store_dwordx4 v[180:183], v42, s[48:51], s66 offen offset:1024 sc1\n"
    "v_mfma_f32_16x16x32_bf16 v[148:151], v[244:247], v[64:67], v[148:151]\n"
    "buffer_load_dwordx4 v12, s[44:47], s69 offen offset:2048 lds\n"
    "s_waitcnt lgkmcnt(0)\n"
    "v_mfma_f32_16x16x32_bf16 v[152:155], v[240:243], v[80:83], v[152:155]\n"
    "ds_read_b128 v[64:67], v14 offset:4096\n"
    "v_mfma_f32_16x16x32_bf16 v[152:155], v[248:251], v[84:87], v[152:155]\n"
    "ds_read_b128 v[68:71], v15 offset:4096\n"
    "v_mfma_f32_16x16x32_bf16 v[152:155], v[208:211], v[88:91], v[152:155]\n"
    "ds_read_b128 v[72:75], v14 offset:36864\n"
    "v_maximum3_f32 v144, v144, 0, 0\n"
    "v_maximum3_f32 v145, v145, 0, 0\n"
    "v_mfma_f32_16x16x32_bf16 v[152:155], v[216:219], v[92:95], v[152:155]\n"
    "ds_read_b128 v[76:79], v15 offset:36864\n"
    "v_cvt_pk_bf16_f32 v192, v144, v145\n"
    "v_maximum3_f32 v146, v146, 0, 0\n"
    "v_mfma_f32_16x16x32_bf16 v[156:159], v[220:223], v[92:95], v[156:159]\n"
    "v_maximum3_f32 v147, v147, 0, 0\n"
    "v_cvt_pk_bf16_f32 v193, v146, v147\n"
    "v_maximum3_f32 v148, v148, 0, 0\n"
    "v_mfma_f32_16x16x32_bf16 v[156:159], v[212:215], v[88:91], v[156:159]\n"
    "v_maximum3_f32 v149, v149, 0, 0\n"
    "v_cvt_pk_bf16_f32 v194, v148, v149\n"
    "v_maximum3_f32 v150, v150, 0, 0\n"
    "v_mfma_f32_16x16x32_bf16 v[156:159], v[252:255], v[84:87], v[156:159]\n"
    "v_maximum3_f32 v151, v151, 0, 0\n"
    "v_cvt_pk_bf16_f32 v195, v150, v151\n"
    "buffer_store_dwordx4 v[192:195], v44, s[48:51], s66 offen offset:0 sc1\n"
    "v_mfma_f32_16x16x32_bf16 v[156:159], v[244:247], v[80:83], v[156:159]\n"
    "buffer_load_dwordx4 v13, s[44:47], s69 offen offset:3072 lds\n"
    "s_waitcnt lgkmcnt(0)\n"
    "v_mfma_f32_16x16x32_bf16 v[128:131], v[240:243], v[64:67], v[128:131]\n"
    "ds_read_b128 v[80:83], v14 offset:6144\n"
    "v_mfma_f32_16x16x32_bf16 v[128:131], v[248:251], v[68:71], v[128:131]\n"
    "ds_read_b128 v[84:87], v15 offset:6144\n"
    "v_mfma_f32_16x16x32_bf16 v[128:131], v[208:211], v[72:75], v[128:131]\n"
    "ds_read_b128 v[88:91], v14 offset:38912\n"
    "v_maximum3_f32 v152, v152, 0, 0\n"
    "v_maximum3_f32 v153, v153, 0, 0\n"
    "v_mfma_f32_16x16x32_bf16 v[128:131], v[216:219], v[76:79], v[128:131]\n"
    "ds_read_b128 v[92:95], v15 offset:38912\n"
    "v_cvt_pk_bf16_f32 v196, v152, v153\n"
    "v_maximum3_f32 v154, v154, 0, 0\n"
    "v_mfma_f32_16x16x32_bf16 v[132:135], v[220:223], v[76:79], v[132:135]\n"
    "v_maximum3_f32 v155, v155, 0, 0\n"
    "v_cvt_pk_bf16_f32 v197, v154, v155\n"
    "v_maximum3_f32 v156, v156, 0, 0\n"
    "v_mfma_f32_16x16x32_bf16 v[132:135], v[212:215], v[72:75], v[132:135]\n"
    "v_maximum3_f32 v157, v157, 0, 0\n"
    "v_cvt_pk_bf16_f32 v198, v156, v157\n"
    "v_maximum3_f32 v158, v158, 0, 0\n"
    "v_mfma_f32_16x16x32_bf16 v[132:135], v[252:255], v[68:71], v[132:135]\n"
    "v_maximum3_f32 v159, v159, 0, 0\n"
    "v_cvt_pk_bf16_f32 v199, v158, v159\n"
    "buffer_store_dwordx4 v[196:199], v44, s[48:51], s66 offen offset:1024 sc1\n"
    "v_mfma_f32_16x16x32_bf16 v[132:135], v[244:247], v[64:67], v[132:135]\n"
    "s_add_u32 m0, s29, 131072\n"
    "s_nop 0\n"
    "buffer_load_dwordx4 v10, s[44:47], s70 offen offset:0 lds\n"
    "s_waitcnt lgkmcnt(0)\n"
    "v_mfma_f32_16x16x32_bf16 v[136:139], v[240:243], v[80:83], v[136:139]\n"
    "ds_read_b128 v[64:67], v14 offset:12288\n"
    "v_mfma_f32_16x16x32_bf16 v[136:139], v[248:251], v[84:87], v[136:139]\n"
    "ds_read_b128 v[68:71], v15 offset:12288\n"
    "v_maximum3_f32 v128, v128, 0, 0\n"
    "v_maximum3_f32 v129, v129, 0, 0\n"
    "v_mfma_f32_16x16x32_bf16 v[136:139], v[208:211], v[88:91], v[136:139]\n"
    "ds_read_b128 v[72:75], v14 offset:45056\n"
    "v_cvt_pk_bf16_f32 v184, v128, v129\n"
    "v_maximum3_f32 v130, v130, 0, 0\n"
    "v_mfma_f32_16x16x32_bf16 v[136:139], v[216:219], v[92:95], v[136:139]\n"
    "ds_read_b128 v[76:79], v15 offset:45056\n"
    "v_maximum3_f32 v131, v131, 0, 0\n"
    "v_cvt_pk_bf16_f32 v185, v130, v131\n"
    "v_mfma_f32_16x16x32_bf16 v[140:143], v[220:223], v[92:95], v[140:143]\n"
    "v_maximum3_f32 v132, v132, 0, 0\n"
    "v_maximum3_f32 v133, v133, 0, 0\n"
    "v_cvt_pk_bf16_f32 v186, v132, v133\n"
    "v_mfma_f32_16x16x32_bf16 v[140:143], v[212:215], v[88:91], v[140:143]\n"
    "v_maximum3_f32 v134, v134, 0, 0\n"
    "v_maximum3_f32 v135, v135, 0, 0\n"
    "v_cvt_pk_bf16_f32 v187, v134, v135\n"
    "v_mfma_f32_16x16x32_bf16 v[140:143], v[252:255], v[84:87], v[140:143]\n"
    "buffer_store_dwordx4 v[184:187], v43, s[48:51], s66 offen offset:0 sc1\n"
    "buffer_load_dwordx4 v11, s[44:47], s70 offen offset:1024 lds\n"
    "v_mfma_f32_16x16x32_bf16 v[140:143], v[244:247], v[80:83], v[140:143]\n"
    "s_waitcnt lgkmcnt(0)\n"
    "v_mfma_f32_16x16x32_bf16 v[160:163], v[240:243], v[64:67], v[160:163]\n"
    "ds_read_b128 v[80:83], v14 offset:14336\n"
    "v_mfma_f32_16x16x32_bf16 v[160:163], v[248:251], v[68:71], v[160:163]\n"
    "ds_read_b128 v[84:87], v15 offset:14336\n"
    "v_mfma_f32_16x16x32_bf16 v[160:163], v[208:211], v[72:75], v[160:163]\n"
    "ds_read_b128 v[88:91], v14 offset:47104\n"
    "v_mfma_f32_16x16x32_bf16 v[160:163], v[216:219], v[76:79], v[160:163]\n"
    "ds_read_b128 v[92:95], v15 offset:47104\n"
    "v_maximum3_f32 v136, v136, 0, 0\n"
    "v_maximum3_f32 v137, v137, 0, 0\n"
    "v_mfma_f32_16x16x32_bf16 v[164:167], v[220:223], v[76:79], v[164:167]\n"
    "v_cvt_pk_bf16_f32 v188, v136, v137\n"
    "v_maximum3_f32 v138, v138, 0, 0\n"
    "v_maximum3_f32 v139, v139, 0, 0\n"
    "v_mfma_f32_16x16x32_bf16 v[164:167], v[212:215], v[72:75], v[164:167]\n"
    "v_cvt_pk_bf16_f32 v189, v138, v139\n"
    "v_maximum3_f32 v140, v140, 0, 0\n"
    "v_maximum3_f32 v141, v141, 0, 0\n"
    "v_mfma_f32_16x16x32_bf16 v[164:167], v[252:255], v[68:71], v[164:167]\n"
    "v_cvt_pk_bf16_f32 v190, v140, v141\n"
    "v_maximum3_f32 v142, v142, 0, 0\n"
    "v_maximum3_f32 v143, v143, 0, 0\n"
    "v_mfma_f32_16x16x32_bf16 v[164:167], v[244:247], v[64:67], v[164:167]\n"
    "v_cvt_pk_bf16_f32 v191, v142, v143\n"
    "buffer_store_dwordx4 v[188:191], v43, s[48:51], s66 offen offset:1024 sc1\n"
    "buffer_load_dwordx4 v12, s[44:47], s70 offen offset:2048 lds\n"
    "s_waitcnt lgkmcnt(0)\n"
    "v_mfma_f32_16x16x32_bf16 v[168:171], v[240:243], v[80:83], v[168:171]\n"
    "v_mfma_f32_16x16x32_bf16 v[168:171], v[248:251], v[84:87], v[168:171]\n"
    "v_mfma_f32_16x16x32_bf16 v[168:171], v[208:211], v[88:91], v[168:171]\n"
    "v_mfma_f32_16x16x32_bf16 v[168:171], v[216:219], v[92:95], v[168:171]\n"
    "v_maximum3_f32 v160, v160, 0, 0\n"
    "v_maximum3_f32 v161, v161, 0, 0\n"
    "v_cvt_pk_bf16_f32 v200, v160, v161\n"
    "v_mfma_f32_16x16x32_bf16 v[172:175], v[220:223], v[92:95], v[172:175]\n"
    "v_maximum3_f32 v162, v162, 0, 0\n"
    "v_maximum3_f32 v163, v163, 0, 0\n"
    "v_cvt_pk_bf16_f32 v201, v162, v163\n"
    "v_mfma_f32_16x16x32_bf16 v[172:175], v[212:215], v[88:91], v[172:175]\n"
    "v_maximum3_f32 v164, v164, 0, 0\n"
    "v_maximum3_f32 v165, v165, 0, 0\n"
    "v_cvt_pk_bf16_f32 v202, v164, v165\n"
    "v_mfma_f32_16x16x32_bf16 v[172:175], v[252:255], v[84:87], v[172:175]\n"
    "v_maximum3_f32 v166, v166, 0, 0\n"
    "v_maximum3_f32 v167, v167, 0, 0\n"
    "v_cvt_pk_bf16_f32 v203, v166, v167\n"
    "v_mfma_f32_16x16x32_bf16 v[172:175], v[244:247], v[80:83], v[172:175]\n"
    "buffer_store_dwordx4 v[200:203], v45, s[48:51], s66 offen offset:0 sc1\n"
    "buffer_load_dwordx4 v13, s[44:47], s70 offen offset:3072 lds\n"
    "; ---- phase 1 MFMAs done\n"
    "s_barrier\n"
    "s_nop 4\n"
    "v_maximum3_f32 v168, v168, 0, 0\n"
    "v_maximum3_f32 v169, v169, 0, 0\n"
    "v_cvt_pk_bf16_f32 v204, v168, v169\n"
    "v_maximum3_f32 v170, v170, 0, 0\n"
    "v_maximum3_f32 v171, v171, 0, 0\n"
    "v_cvt_pk_bf16_f32 v205, v170, v171\n"
    "v_maximum3_f32 v172, v172, 0, 0\n"
    "v_maximum3_f32 v173, v173, 0, 0\n"
    "v_cvt_pk_bf16_f32 v206, v172, v173\n"
    "v_maximum3_f32 v174, v174, 0, 0\n"
    "v_maximum3_f32 v175, v175, 0, 0\n"
    "v_cvt_pk_bf16_f32 v207, v174, v175\n"
    "buffer_store_dwordx4 v[204:207], v45, s[48:51], s66 offen offset:1024 sc1\n"
    "s_add_u32 m0, s29, 135168\n"
    "s_nop 0\n"
    "buffer_load_dwordx4 v10, s[44:47], s71 offen offset:0 lds\n"
    "; ---- all h fragments issued (sc1)\n"
    "s_waitcnt vmcnt(25)\n"
    "ds_read_b64_tr_b16 a[0:1], v30 offset:0\n"
    "ds_read_b64_tr_b16 a[2:3], v30 offset:2048\n"
    "ds_read_b64_tr_b16 a[4:5], v31 offset:0\n"
    "ds_read_b64_tr_b16 a[6:7], v31 offset:2048\n"
    "ds_read_b64_tr_b16 a[8:9], v32 offset:0\n"
    "ds_read_b64_tr_b16 a[10:11], v32 offset:2048\n"
    "ds_read_b64_tr_b16 a[12:13], v33 offset:0\n"
    "ds_read_b64_tr_b16 a[14:15], v33 offset:2048\n"
    "s_waitcnt vmcnt(21)\n"
    "ds_read_b64_tr_b16 a[16:17], v30 offset:4096\n"
    "ds_read_b64_tr_b16 a[18:19], v30 offset:6144\n"
    "ds_read_b64_tr_b16 a[20:21], v31 offset:4096\n"
    "ds_read_b64_tr_b16 a[22:23], v31 offset:6144\n"
    "ds_read_b64_tr_b16 a[24:25], v32 offset:4096\n"
    "ds_read_b64_tr_b16 a[26:27], v32 offset:6144\n"
    "ds_read_b64_tr_b16 a[28:29], v33 offset:4096\n"
    "s_waitcnt lgkmcnt(8)\n"
    "ds_read_b64_tr_b16 a[30:31], v33 offset:6144\n"
    "s_waitcnt vmcnt(9)\n"
    "s_mov_b64 exec, 0xff\n"
    "buffer_store_dwordx4 v[56:59], v63, s[48:51], s60 offen offset:0 sc1\n"
    "s_mov_b64 exec, -1\n"
    "; ---- first flag published\n"
    "ds_read_b64_tr_b16 a[32:33], v30 offset:32768\n"
    "ds_read_b64_tr_b16 a[34:35], v30 offset:34816\n"
    "ds_read_b64_tr_b16 a[36:37], v31 offset:32768\n"
    "ds_read_b64_tr_b16 a[38:39], v31 offset:34816\n"
    "ds_read_b64_tr_b16 a[40:41], v32 offset:32768\n"
    "ds_read_b64_tr_b16 a[42:43], v32 offset:34816\n"
    "s_waitcnt lgkmcnt(8)\n"
    "ds_read_b64_tr_b16 a[44:45], v33 offset:32768\n"
    "ds_read_b64_tr_b16 a[46:47], v33 offset:34816\n"
    "v_mfma_f32_16x16x32_bf16 v[208:211], a[0:3], v[176:179], 0\n"
    "v_mfma_f32_16x16x32_bf16 v[212:215], a[4:7], v[176:179], 0\n"
    "v_mfma_f32_16x16x32_bf16 v[216:219], a[8:11], v[176:179], 0\n"
    "v_mfma_f32_16x16x32_bf16 v[220:223], a[12:15], v[176:179], 0\n"
    "v_mfma_f32_16x16x32_bf16 v[224:227], a[0:3], v[180:183], 0\n"
    "v_mfma_f32_16x16x32_bf16 v[228:231], a[4:7], v[180:183], 0\n"
    "v_mfma_f32_16x16x32_bf16 v[232:235], a[8:11], v[180:183], 0\n"
    "v_mfma_f32_16x16x32_bf16 v[236:239], a[12:15], v[180:183], 0\n"
    "v_mfma_f32_16x16x32_bf16 a[224:227], a[0:3], v[184:187], 0\n"
    "v_mfma_f32_16x16x32_bf16 a[228:231], a[4:7], v[184:187], 0\n"
    "v_mfma_f32_16x16x32_bf16 a[232:235], a[8:11], v[184:187], 0\n"
    "v_mfma_f32_16x16x32_bf16 a[236:239], a[12:15], v[184:187], 0\n"
    "v_mfma_f32_16x16x32_bf16 a[240:243], a[0:3], v[188:191], 0\n"
    "v_mfma_f32_16x16x32_bf16 a[244:247], a[4:7], v[188:191], 0\n"
    "v_mfma_f32_16x16x32_bf16 a[248:251], a[8:11], v[188:191], 0\n"
    "v_mfma_f32_16x16x32_bf16 a[252:255], a[12:15], v[188:191], 0\n"
    "s_waitcnt lgkmcnt(0)\n"
    "; ---- remaining w2 slices, second flag, poll\n"
    "s_add_u32 s56, s60, 4096\n"
    "s_cmp_eq_u32 s19, 0\n"
    "s_cselect_b32 s61, s61, s56\n"
    "buffer_load_dwordx4 v11, s[44:47], s71 offen offset:1024 lds\n"
    "ds_read_b64_tr_b16 a[48:49], v30 offset:36864\n"
    "ds_read_b64_tr_b16 a[50:51], v30 offset:38912\n"
    "ds_read_b64_tr_b16 a[52:53], v31 offset:36864\n"
    "buffer_load_dwordx4 v12, s[44:47], s71 offen offset:2048 lds\n"
    "ds_read_b64_tr_b16 a[54:55], v31 offset:38912\n"
    "ds_read_b64_tr_b16 a[56:57], v32 offset:36864\n"
    "ds_read_b64_tr_b16 a[58:59], v32 offset:38912\n"
    "buffer_load_dwordx4 v13, s[44:47], s71 offen offset:3072 lds\n"
    "s_add_u32 m0, s29, 0\n"
    "ds_read_b64_tr_b16 a[60:61], v33 offset:36864\n"
    "ds_read_b64_tr_b16 a[62:63], v33 offset:38912\n"
    "buffer_load_dwordx4 v10, s[44:47], s72 offen offset:0 lds\n"
    "buffer_load_dwordx4 v11, s[44:47], s72 offen offset:1024 lds\n"
    "buffer_load_dwordx4 v12, s[44:47], s72 offen offset:2048 lds\n"
    "buffer_load_dwordx4 v13, s[44:47], s72 offen offset:3072 lds\n"
    "s_add_u32 m0, s29, 4096\n"
    "s_nop 0\n"
    "buffer_load_dwordx4 v10, s[44:47], s73 offen offset:0 lds\n"
    "buffer_load_dwordx4 v11, s[44:47], s73 offen offset:1024 lds\n"
    "s_waitcnt vmcnt(12)\n"
    "buffer_load_dwordx4 v12, s[44:47], s73 offen offset:2048 lds\n"
    "ds_read_b64_tr_b16 a[64:65], v34 offset:0\n"
    "ds_read_b64_tr_b16 a[66:67], v34 offset:2048\n"
    "ds_read_b64_tr_b16 a[68:69], v35 offset:0\n"
    "buffer_load_dwordx4 v13, s[44:47], s73 offen offset:3072 lds\n"
    "s_add_u32 m0, s29, 32768\n"
    "ds_read_b64_tr_b16 a[70:71], v35 offset:2048\n"
    "ds_read_b64_tr_b16 a[72:73], v36 offset:0\n"
    "ds_read_b64_tr_b16 a[74:75], v36 offset:2048\n"
    "buffer_load_dwordx4 v10, s[44:47], s74 offen offset:0 lds\n"
    "ds_read_b64_tr_b16 a[76:77], v37 offset:0\n"
    "s_waitcnt lgkmcnt(8)\n"
    "ds_read_b64_tr_b16 a[78:79], v37 offset:2048\n"
    "buffer_load_dwordx4 v11, s[44:47], s74 offen offset:1024 lds\n"
    "buffer_load_dwordx4 v12, s[44:47], s74 offen offset:2048 lds\n"
    "buffer_load_dwordx4 v13, s[44:47], s74 offen offset:3072 lds\n"
    "s_add_u32 m0, s29, 36864\n"
    "s_waitcnt vmcnt(12)\n"
    "buffer_load_dwordx4 v10, s[44:47], s75 offen offset:0 lds\n"
    "ds_read_b64_tr_b16 a[80:81], v34 offset:4096\n"
    "ds_read_b64_tr_b16 a[82:83], v34 offset:6144\n"
    "ds_read_b64_tr_b16 a[84:85], v35 offset:4096\n"
    "buffer_load_dwordx4 v11, s[44:47], s75 offen offset:1024 lds\n"
    "ds_read_b64_tr_b16 a[86:87], v35 offset:6144\n"
    "ds_read_b64_tr_b16 a[88:89], v36 offset:4096\n"
    "ds_read_b64_tr_b16 a[90:91], v36 offset:6144\n"
    "buffer_load_dwordx4 v12, s[44:47], s75 offen offset:2048 lds\n"
    "s_waitcnt lgkmcnt(8)\n"
    "ds_read_b64_tr_b16 a[92:93], v37 offset:4096\n"
    "ds_read_b64_tr_b16 a[94:95], v37 offset:6144\n"
    "buffer_load_dwordx4 v13, s[44:47], s75 offen offset:3072 lds\n"
    "s_add_u32 m0, s29, 65536\n"
    "s_waitcnt vmcnt(12)\n"
    "buffer_load_dwordx4 v10, s[44:47], s76 offen offset:0 lds\n"
    "ds_read_b64_tr_b16 a[96:97], v26 offset:0\n"
    "ds_read_b64_tr_b16 a[98:99], v26 offset:2048\n"
    "ds_read_b64_tr_b16 a[100:101], v27 offset:0\n"
    "buffer_load_dwordx4 v11, s[44:47], s76 offen offset:1024 lds\n"
    "ds_read_b64_tr_b16 a[102:103], v27 offset:2048\n"
    "ds_read_b64_tr_b16 a[104:105], v28 offset:0\n"
    "s_waitcnt lgkmcnt(8)\n"
    "ds_read_b64_tr_b16 a[106:107], v28 offset:2048\n"
    "buffer_load_dwordx4 v12, s[44:47], s76 offen offset:2048 lds\n"
    "ds_read_b64_tr_b16 a[108:109], v29 offset:0\n"
    "ds_read_b64_tr_b16 a[110:111], v29 offset:2048\n"
    "buffer_load_dwordx4 v13, s[44:47], s76 offen offset:3072 lds\n"
    "s_add_u32 m0, s29, 69632\n"
    "s_waitcnt vmcnt(12)\n"
    "buffer_load_dwordx4 v10, s[44:47], s77 offen offset:0 lds\n"
    "ds_read_b64_tr_b16 a[112:113], v26 offset:4096\n"
    "ds_read_b64_tr_b16 a[114:115], v26 offset:6144\n"
    "ds_read_b64_tr_b16 a[116:117], v27 offset:4096\n"
    "s_mov_b64 exec, 0xff\n"
    "buffer_store_dwordx4 v[56:59], v63, s[48:51], s60 offen offset:2048 sc1\n"
    "s_mov_b64 exec, -1\n"
    "buffer_load_dwordx2 v[40:41], v46, s[48:51], s62 offen offset:0 sc1\n"
    "ds_read_b64_tr_b16 a[118:119], v27 offset:6144\n"
    "s_waitcnt lgkmcnt(8)\n"
    "ds_read_b64_tr_b16 a[120:121], v28 offset:4096\n"
    "ds_read_b64_tr_b16 a[122:123], v28 offset:6144\n"
    "buffer_load_dwordx4 v11, s[44:47], s77 offen offset:1024 lds\n"
    "ds_read_b64_tr_b16 a[124:125], v29 offset:4096\n"
    "ds_read_b64_tr_b16 a[126:127], v29 offset:6144\n"
    "s_waitcnt vmcnt(12)\n"
    "buffer_load_dwordx4 v12, s[44:47], s77 offen offset:2048 lds\n"
    "ds_read_b64_tr_b16 a[128:129], v26 offset:32768\n"
    "ds_read_b64_tr_b16 a[130:131], v26 offset:34816\n"
    "ds_read_b64_tr_b16 a[132:133], v27 offset:32768\n"
    "buffer_load_dwordx4 v13, s[44:47], s77 offen offset:3072 lds\n"
    "s_add_u32 m0, s29, 98304\n"
    "s_waitcnt lgkmcnt(8)\n"
    "ds_read_b64_tr_b16 a[134:135], v27 offset:34816\n"
    "ds_read_b64_tr_b16 a[136:137], v28 offset:32768\n"
    "ds_read_b64_tr_b16 a[138:139], v28 offset:34816\n"
    "buffer_load_dwordx4 v10, s[44:47], s78 offen offset:0 lds\n"
    "ds_read_b64_tr_b16 a[140:141], v29 offset:32768\n"
    "ds_read_b64_tr_b16 a[142:143], v29 offset:34816\n"
    "buffer_load_dwordx4 v11, s[44:47], s78 offen offset:1024 lds\n"
    "s_waitcnt vmcnt(12)\n"
    "buffer_load_dwordx4 v12, s[44:47], s78 offen offset:2048 lds\n"
    "ds_read_b64_tr_b16 a[144:145], v26 offset:36864\n"
    "ds_read_b64_tr_b16 a[146:147], v26 offset:38912\n"
    "s_waitcnt lgkmcnt(8)\n"
    "ds_read_b64_tr_b16 a[148:149], v27 offset:36864\n"
    "buffer_load_dwordx4 v13, s[44:47], s78 offen offset:3072 lds\n"
    "s_add_u32 m0, s29, 102400\n"
    "ds_read_b64_tr_b16 a[150:151], v27 offset:38912\n"
    "ds_read_b64_tr_b16 a[152:153], v28 offset:36864\n"
    "ds_read_b64_tr_b16 a[154:155], v28 offset:38912\n"
    "buffer_load_dwordx4 v10, s[44:47], s79 offen offset:0 lds\n"
    "ds_read_b64_tr_b16 a[156:157], v29 offset:36864\n"
    "ds_read_b64_tr_b16 a[158:159], v29 offset:38912\n"
    "buffer_load_dwordx4 v11, s[44:47], s79 offen offset:1024 lds\n"
    "s_waitcnt vmcnt(12)\n"
    "buffer_load_dwordx4 v12, s[44:47], s79 offen offset:2048 lds\n"
    "ds_read_b64_tr_b16 a[160:161], v30 offset:0\n"
    "s_waitcnt lgkmcnt(8)\n"
    "ds_read_b64_tr_b16 a[162:163], v30 offset:2048\n"
    "ds_read_b64_tr_b16 a[164:165], v31 offset:0\n"
    "buffer_load_dwordx4 v13, s[44:47], s79 offen offset:3072 lds\n"
    "ds_read_b64_tr_b16 a[166:167], v31 offset:2048\n"
    "ds_read_b64_tr_b16 a[168:169], v32 offset:0\n"
    "ds_read_b64_tr_b16 a[170:171], v32 offset:2048\n"
    "s_waitcnt vmcnt(11)\n"
    "v_cmp_eq_u64 vcc, v[40:41], v[56:57]\n"
    "s_cmp_eq_u64 vcc, exec\n"
    "s_cbranch_scc0 .Lpoll_retry0_%=\n"
    ".Lpoll_ok0_%=:\n"
    "; ---- first-part flags of all producers seen: phase 2 stream\n"
    "buffer_load_dwordx4 v[64:67], v42, s[48:51], s67 offen offset:0 sc1\n"
    "ds_read_b64_tr_b16 a[172:173], v33 offset:0\n"
    "ds_read_b64_tr_b16 a[174:175], v33 offset:2048\n"
    "buffer_load_dwordx4 v[68:71], v42, s[48:51], s68 offen offset:0 sc1\n"
    "buffer_load_dwordx4 v[72:75], v42, s[48:51], s67 offen offset:1024 sc1\n"
    "buffer_load_dwordx4 v[76:79], v42, s[48:51], s68 offen offset:1024 sc1\n"
    "s_waitcnt vmcnt(12)\n"
    "buffer_load_dwordx4 v[80:83], v42, s[48:51], s69 offen offset:0 sc1\n"
    "s_waitcnt lgkmcnt(8)\n"
    "ds_read_b64_tr_b16 a[176:177], v30 offset:4096\n"
    "ds_read_b64_tr_b16 a[178:179], v30 offset:6144\n"
    "ds_read_b64_tr_b16 a[180:181], v31 offset:4096\n"
    "buffer_load_dwordx4 v[84:87], v42, s[48:51], s70 offen offset:0 sc1\n"
    "ds_read_b64_tr_b16 a[182:183], v31 offset:6144\n"
    "ds_read_b64_tr_b16 a[184:185], v32 offset:4096\n"
    "ds_read_b64_tr_b16 a[186:187], v32 offset:6144\n"
    "buffer_load_dwordx4 v[88:91], v42, s[48:51], s69 offen offset:1024 sc1\n"
    "ds_read_b64_tr_b16 a[188:189], v33 offset:4096\n"
    "s_waitcnt lgkmcnt(8)\n"
    "ds_read_b64_tr_b16 a[190:191], v33 offset:6144\n"
    "buffer_load_dwordx4 v[92:95], v42, s[48:51], s70 offen offset:1024 sc1\n"
    "s_waitcnt vmcnt(12)\n"
    "buffer_load_dwordx4 v[96:99], v42, s[48:51], s71 offen offset:0 sc1\n"
    "ds_read_b64_tr_b16 a[192:193], v30 offset:32768\n"
    "ds_read_b64_tr_b16 a[194:195], v30 offset:34816\n"
    "ds_read_b64_tr_b16 a[196:197], v31 offset:32768\n"
    "buffer_load_dwordx4 v[100:103], v42, s[48:51], s72 offen offset:0 sc1\n"
    "ds_read_b64_tr_b16 a[198:199], v31 offset:34816\n"
    "ds_read_b64_tr_b16 a[200:201], v32 offset:32768\n"
    "ds_read_b64_tr_b16 a[202:203], v32 offset:34816\n"
    "buffer_load_dwordx4 v[104:107], v42, s[48:51], s71 offen offset:1024 sc1\n"
    "s_waitcnt lgkmcnt(8)\n"
    "ds_read_b64_tr_b16 a[204:205], v33 offset:32768\n"
    "ds_read_b64_tr_b16 a[206:207], v33 offset:34816\n"
    "buffer_load_dwordx4 v[108:111], v42, s[48:51], s72 offen offset:1024 sc1\n"
    "s_waitcnt vmcnt(12)\n"
    "buffer_load_dwordx4 v[112:115], v42, s[48:51], s73 offen offset:0 sc1\n"
    "ds_read_b64_tr_b16 a[208:209], v30 offset:36864\n"
    "ds_read_b64_tr_b16 a[210:211], v30 offset:38912\n"
    "ds_read_b64_tr_b16 a[212:213], v31 offset:36864\n"
    "s_waitcnt vmcnt(11)\n"
    "v_mfma_f32_16x16x32_bf16 v[208:211], a[16:19], v[64:67], v[208:211]\n"
    "ds_read_b64_tr_b16 a[214:215], v31 offset:38912\n"
    "ds_read_b64_tr_b16 a[216:217], v32 offset:36864\n"
    "v_mfma_f32_16x16x32_bf16 v[208:211], a[32:35], v[68:71], v[208:211]\n"
    "buffer_load_dwordx4 v[116:119], v42, s[48:51], s74 offen offset:0 sc1\n"
    "v_mfma_f32_16x16x32_bf16 v[212:215], a[36:39], v[68:71], v[212:215]\n"
    "s_waitcnt lgkmcnt(8)\n"
    "ds_read_b64_tr_b16 a[218:219], v32 offset:38912\n"
    "ds_read_b64_tr_b16 a[220:221], v33 offset:36864\n"
    "v_mfma_f32_16x16x32_bf16 v[212:215], a[20:23], v[64:67], v[212:215]\n"
    "ds_read_b64_tr_b16 a[222:223], v33 offset:38912\n"
    "v_mfma_f32_16x16x32_bf16 v[216:219], a[24:27], v[64:67], v[216:219]\n"
    "v_mfma_f32_16x16x32_bf16 v[216:219], a[40:43], v[68:71], v[216:219]\n"
    "buffer_load_dwordx4 v[120:123], v42, s[48:51], s73 offen offset:1024 sc1\n"
    "v_mfma_f32_16x16x32_bf16 v[220:223], a[44:47], v[68:71], v[220:223]\n"
    "v_mfma_f32_16x16x32_bf16 v[220:223], a[28:31], v[64:67], v[220:223]\n"
    "s_waitcnt vmcnt(11)\n"
    "v_mfma_f32_16x16x32_bf16 v[224:227], a[16:19], v[72:75], v[224:227]\n"
    "v_mfma_f32_16x16x32_bf16 v[224:227], a[32:35], v[76:79], v[224:227]\n"
    "buffer_load_dwordx2 v[40:41], v46, s[48:51], s62 offen offset:2048 sc1\n"
    "v_mfma_f32_16x16x32_bf16 v[228:231], a[36:39], v[76:79], v[228:231]\n"
    "v_mfma_f32_16x16x32_bf16 v[228:231], a[20:23], v[72:75], v[228:231]\n"
    "v_mfma_f32_16x16x32_bf16 v[232:235], a[24:27], v[72:75], v[232:235]\n"
    "v_mfma_f32_16x16x32_bf16 v[232:235], a[40:43], v[76:79], v[232:235]\n"
    "buffer_load_dwordx4 v[124:127], v42, s[48:51], s74 offen offset:1024 sc1\n"
    "v_mfma_f32_16x16x32_bf16 v[236:239], a[44:47], v[76:79], v[236:239]\n"
    "v_mfma_f32_16x16x32_bf16 v[236:239], a[28:31], v[72:75], v[236:239]\n"
    "s_waitcnt vmcnt(11)\n"
    "v_mfma_f32_16x16x32_bf16 v[208:211], a[48:51], v[80:83], v[208:211]\n"
    "v_mfma_f32_16x16x32_bf16 v[208:211], a[64:67], v[84:87], v[208:211]\n"
    "buffer_load_dwordx4 v[128:131], v42, s[48:51], s75 offen offset:0 sc1\n"
    "v_mfma_f32_16x16x32_bf16 v[212:215], a[68:71], v[84:87], v[212:215]\n"
    "v_mfma_f32_16x16x32_bf16 v[212:215], a[52:55], v[80:83], v[212:215]\n"
    "v_mfma_f32_16x16x32_bf16 v[216:219], a[56:59], v[80:83], v[216:219]\n"
    "v_mfma_f32_16x16x32_bf16 v[216:219], a[72:75], v[84:87], v[216:219]\n"
    "buffer_load_dwordx4 v[132:135], v42, s[48:51], s76 offen offset:0 sc1\n"
    "v_mfma_f32_16x16x32_bf16 v[220:223], a[76:79], v[84:87], v[220:223]\n"
    "v_mfma_f32_16x16x32_bf16 v[220:223], a[60:63], v[80:83], v[220:223]\n"
    "s_waitcnt vmcnt(11)\n"
    "v_mfma_f32_16x16x32_bf16 v[224:227], a[48:51], v[88:91], v[224:227]\n"
    "v_mfma_f32_16x16x32_bf16 v[224:227], a[64:67], v[92:95], v[224:227]\n"
    "buffer_load_dwordx4 v[64:67], v42, s[48:51], s75 offen offset:1024 sc1\n"
    "v_mfma_f32_16x16x32_bf16 v[228:231], a[68:71], v[92:95], v[228:231]\n"
    "v_mfma_f32_16x16x32_bf16 v[228:231], a[52:55], v[88:91], v[228:231]\n"
    "v_mfma_f32_16x16x32_bf16 v[232:235], a[56:59], v[88:91], v[232:235]\n"
    "v_mfma_f32_16x16x32_bf16 v[232:235], a[72:75], v[92:95], v[232:235]\n"
    "buffer_load_dwordx4 v[68:71], v42, s[48:51], s76 offen offset:1024 sc1\n"
    "v_mfma_f32_16x16x32_bf16 v[236:239], a[76:79], v[92:95], v[236:239]\n"
    "v_mfma_f32_16x16x32_bf16 v[236:239], a[60:63], v[88:91], v[236:239]\n"
    "s_waitcnt vmcnt(11)\n"
    "v_mfma_f32_16x16x32_bf16 v[208:211], a[80:83], v[96:99], v[208:211]\n"
    "v_mfma_f32_16x16x32_bf16 v[208:211], a[96:99], v[100:103], v[208:211]\n"
    "buffer_load_dwordx4 v[72:75], v42, s[48:51], s77 offen offset:0 sc1\n"
    "v_mfma_f32_16x16x32_bf16 v[212:215], a[100:103], v[100:103], v[212:215]\n"
    "v_mfma_f32_16x16x32_bf16 v[212:215], a[84:87], v[96:99], v[212:215]\n"
    "v_mfma_f32_16x16x32_bf16 v[216:219], a[88:91], v[96:99], v[216:219]\n"
    "v_mfma_f32_16x16x32_bf16 v[216:219], a[104:107], v[100:103], v[216:219]\n"
    "buffer_load_dwordx4 v[76:79], v42, s[48:51], s78 offen offset:0 sc1\n"
    "v_mfma_f32_16x16x32_bf16 v[220:223], a[108:111], v[100:103], v[220:223]\n"
    "v_mfma_f32_16x16x32_bf16 v[220:223], a[92:95], v[96:99], v[220:223]\n"
    "s_waitcnt vmcnt(11)\n"
    "v_mfma_f32_16x16x32_bf16 v[224:227], a[80:83], v[104:107], v[224:227]\n"
    "v_mfma_f32_16x16x32_bf16 v[224:227], a[96:99], v[108:111], v[224:227]\n"
    "buffer_load_dwordx4 v[80:83], v42, s[48:51], s77 offen offset:1024 sc1\n"
    "v_mfma_f32_16x16x32_bf16 v[228:231], a[100:103], v[108:111], v[228:231]\n"
    "v_mfma_f32_16x16x32_bf16 v[228:231], a[84:87], v[104:107], v[228:231]\n"
    "v_mfma_f32_16x16x32_bf16 v[232:235], a[88:91], v[104:107], v[232:235]\n"
    "v_mfma_f32_16x16x32_bf16 v[232:235], a[104:107], v[108:111], v[232:235]\n"
    "buffer_load_dwordx4 v[84:87], v42, s[48:51], s78 offen offset:1024 sc1\n"
    "v_mfma_f32_16x16x32_bf16 v[236:239], a[108:111], v[108:111], v[236:239]\n"
    "v_mfma_f32_16x16x32_bf16 v[236:239], a[92:95], v[104:107], v[236:239]\n"
    "s_waitcnt vmcnt(11)\n"
    "v_mfma_f32_16x16x32_bf16 v[208:211], a[112:115], v[112:115], v[208:211]\n"
    "v_mfma_f32_16x16x32_bf16 v[208:211], a[128:131], v[116:119], v[208:211]\n"
    "buffer_load_dwordx4 v[88:91], v42, s[48:51], s79 offen offset:0 sc1\n"
    "v_mfma_f32_16x16x32_bf16 v[212:215], a[132:135], v[116:119], v[212:215]\n"
    "v_mfma_f32_16x16x32_bf16 v[212:215], a[116:119], v[112:115], v[212:215]\n"
    "v_mfma_f32_16x16x32_bf16 v[216:219], a[120:123], v[112:115], v[216:219]\n"
    "v_mfma_f32_16x16x32_bf16 v[216:219], a[136:139], v[116:119], v[216:219]\n"
    "buffer_load_dwordx4 v[92:95], v42, s[48:51], s79 offen offset:1024 sc1\n"
    "v_mfma_f32_16x16x32_bf16 v[220:223], a[140:143], v[116:119], v[220:223]\n"
    "v_mfma_f32_16x16x32_bf16 v[220:223], a[124:127], v[112:115], v[220:223]\n"
    "s_waitcnt vmcnt(11)\n"
    "v_cmp_eq_u64 vcc, v[40:41], v[56:57]\n"
    "s_cmp_eq_u64 vcc, exec\n"
    "s_cbranch_scc0 .Lpoll_retry1_%=\n"
    ".Lpoll_ok1_%=:\n"
    "; ---- second-part flags seen\n"
    "buffer_load_dwordx4 v[96:99], v43, s[48:51], s67 offen offset:0 sc1\n"
    "s_waitcnt vmcnt(11)\n"
    "v_mfma_f32_16x16x32_bf16 v[224:227], a[112:115], v[120:123], v[224:227]\n"
    "v_mfma_f32_16x16x32_bf16 v[224:227], a[128:131], v[124:127], v[224:227]\n"
    "buffer_load_dwordx4 v[100:103], v43, s[48:51], s68 offen offset:0 sc1\n"
    "v_mfma_f32_16x16x32_bf16 v[228:231], a[132:135], v[124:127], v[228:231]\n"
    "v_mfma_f32_16x16x32_bf16 v[228:231], a[116:119], v[120:123], v[228:231]\n"
    "v_mfma_f32_16x16x32_bf16 v[232:235], a[120:123], v[120:123], v[232:235]\n"
    "v_mfma_f32_16x16x32_bf16 v[232:235], a[136:139], v[124:127], v[232:235]\n"
    "buffer_load_dwordx4 v[104:107], v43, s[48:51], s69 offen offset:0 sc1\n"
    "v_mfma_f32_16x16x32_bf16 v[236:239], a[140:143], v[124:127], v[236:239]\n"
    "v_mfma_f32_16x16x32_bf16 v[236:239], a[124:127], v[120:123], v[236:239]\n"
    "s_waitcnt vmcnt(11)\n"
    "v_mfma_f32_16x16x32_bf16 v[208:211], a[144:147], v[128:131], v[208:211]\n"
    "v_mfma_f32_16x16x32_bf16 v[208:211], a[160:163], v[132:135], v[208:211]\n"
    "buffer_load_dwordx4 v[108:111], v43, s[48:51], s70 offen offset:0 sc1\n"
    "v_mfma_f32_16x16x32_bf16 v[212:215], a[164:167], v[132:135], v[212:215]\n"
    "v_mfma_f32_16x16x32_bf16 v[212:215], a[148:151], v[128:131], v[212:215]\n"
    "v_mfma_f32_16x16x32_bf16 v[216:219], a[152:155], v[128:131], v[216:219]\n"
    "v_mfma_f32_16x16x32_bf16 v[216:219], a[168:171], v[132:135], v[216:219]\n"
    "buffer_load_dwordx4 v[112:115], v43, s[48:51], s71 offen offset:0 sc1\n"
    "v_mfma_f32_16x16x32_bf16 v[220:223], a[172:175], v[132:135], v[220:223]\n"
    "v_mfma_f32_16x16x32_bf16 v[220:223], a[156:159], v[128:131], v[220:223]\n"
    "s_waitcnt vmcnt(11)\n"
    "v_mfma_f32_16x16x32_bf16 v[224:227], a[144:147], v[64:67], v[224:227]\n"
    "v_mfma_f32_16x16x32_bf16 v[224:227], a[160:163], v[68:71], v[224:227]\n"
    "buffer_load_dwordx4 v[116:119], v43, s[48:51], s72 offen offset:0 sc1\n"
    "v_mfma_f32_16x16x32_bf16 v[228:231], a[164:167], v[68:71], v[228:231]\n"
    "v_mfma_f32_16x16x32_bf16 v[228:231], a[148:151], v[64:67], v[228:231]\n"
    "v_mfma_f32_16x16x32_bf16 v[232:235], a[152:155], v[64:67], v[232:235]\n"
    "v_mfma_f32_16x16x32_bf16 v[232:235], a[168:171], v[68:71], v[232:235]\n"
    "buffer_load_dwordx4 v[120:123], v43, s[48:51], s73 offen offset:0 sc1\n"
    "v_mfma_f32_16x16x32_bf16 v[236:239], a[172:175], v[68:71], v[236:239]\n"
    "v_mfma_f32_16x16x32_bf16 v[236:239], a[156:159], v[64:67], v[236:239]\n"
    "s_waitcnt vmcnt(11)\n"
    "v_mfma_f32_16x16x32_bf16 v[208:211], a[176:179], v[72:75], v[208:211]\n"
    "s_waitcnt lgkmcnt(8)\n"
    "v_mfma_f32_16x16x32_bf16 v[208:211], a[192:195], v[76:79], v[208:211]\n"
    "buffer_load_dwordx4 v[124:127], v43, s[48:51], s74 offen offset:0 sc1\n"
    "v_mfma_f32_16x16x32_bf16 v[212:215], a[196:199], v[76:79], v[212:215]\n"
    "v_mfma_f32_16x16x32_bf16 v[212:215], a[180:183], v[72:75], v[212:215]\n"
    "v_mfma_f32_16x16x32_bf16 v[216:219], a[184:187], v[72:75], v[216:219]\n"
    "v_mfma_f32_16x16x32_bf16 v[216:219], a[200:203], v[76:79], v[216:219]\n"
    "buffer_load_dwordx4 v[128:131], v43, s[48:51], s75 offen offset:0 sc1\n"
    "v_mfma_f32_16x16x32_bf16 v[220:223], a[204:207], v[76:79], v[220:223]\n"
    "v_mfma_f32_16x16x32_bf16 v[220:223], a[188:191], v[72:75], v[220:223]\n"
    "s_waitcnt vmcnt(11)\n"
    "v_mfma_f32_16x16x32_bf16 v[224:227], a[176:179], v[80:83], v[224:227]\n"
    "v_mfma_f32_16x16x32_bf16 v[224:227], a[192:195], v[84:87], v[224:227]\n"
    "buffer_load_dwordx4 v[132:135], v43, s[48:51], s76 offen offset:0 sc1\n"
    "v_mfma_f32_16x16x32_bf16 v[228:231], a[196:199], v[84:87], v[228:231]\n"
    "v_mfma_f32_16x16x32_bf16 v[228:231], a[180:183], v[80:83], v[228:231]\n"
    "v_mfma_f32_16x16x32_bf16 v[232:235], a[184:187], v[80:83], v[232:235]\n"
    "v_mfma_f32_16x16x32_bf16 v[232:235], a[200:203], v[84:87], v[232:235]\n"
    "buffer_load_dwordx4 v[64:67], v43, s[48:51], s77 offen offset:0 sc1\n"
    "v_mfma_f32_16x16x32_bf16 v[236:239], a[204:207], v[84:87], v[236:239]\n"
    "v_mfma_f32_16x16x32_bf16 v[236:239], a[188:191], v[80:83], v[236:239]\n"
    "s_waitcnt vmcnt(12)\n"
    "s_waitcnt lgkmcnt(0)\n"
    "v_mfma_f32_16x16x32_bf16 v[208:211], a[208:211], v[88:91], v[208:211]\n"
    "v_mfma_f32_16x16x32_bf16 v[212:215], a[212:215], v[88:91], v[212:215]\n"
    "buffer_load_dwordx4 v[68:71], v43, s[48:51], s78 offen offset:0 sc1\n"
    "v_mfma_f32_16x16x32_bf16 v[216:219], a[216:219], v[88:91], v[216:219]\n"
    "v_mfma_f32_16x16x32_bf16 v[220:223], a[220:223], v[88:91], v[220:223]\n"
    "s_waitcnt vmcnt(12)\n"
    "v_mfma_f32_16x16x32_bf16 v[224:227], a[208:211], v[92:95], v[224:227]\n"
    "v_mfma_f32_16x16x32_bf16 v[228:231], a[212:215], v[92:95], v[228:231]\n"
    "buffer_load_dwordx4 v[72:75], v43, s[48:51], s79 offen offset:0 sc1\n"
    "v_mfma_f32_16x16x32_bf16 v[232:235], a[216:219], v[92:95], v[232:235]\n"
    "v_mfma_f32_16x16x32_bf16 v[236:239], a[220:223], v[92:95], v[236:239]\n"
    "; ---- rows 0..31 complete\n"
    "s_waitcnt vmcnt(11)\n"
    "v_mfma_f32_16x16x32_bf16 a[224:227], a[16:19], v[96:99], a[224:227]\n"
    "ds_write_b128 v47, v[208:211] offset:0\n"
    "v_mfma_f32_16x16x32_bf16 a[224:227], a[32:35], v[100:103], a[224:227]\n"
    "buffer_load_dwordx4 v[76:79], v43, s[48:51], s67 offen offset:1024 sc1\n"
    "v_mfma_f32_16x16x32_bf16 a[228:231], a[36:39], v[100:103], a[228:231]\n"
    "ds_write_b128 v48, v[212:215] offset:0\n"
    "v_mfma_f32_16x16x32_bf16 a[228:231], a[20:23], v[96:99], a[228:231]\n"
    "v_mfma_f32_16x16x32_bf16 a[232:235], a[24:27], v[96:99], a[232:235]\n"
    "ds_write_b128 v47, v[216:219] offset:128\n"
    "v_mfma_f32_16x16x32_bf16 a[232:235], a[40:43], v[100:103], a[232:235]\n"
    "buffer_load_dwordx4 v[80:83], v43, s[48:51], s68 offen offset:1024 sc1\n"
    "v_mfma_f32_16x16x32_bf16 a[236:239], a[44:47], v[100:103], a[236:239]\n"
    "ds_write_b128 v48, v[220:223] offset:128\n"
    "v_mfma_f32_16x16x32_bf16 a[236:239], a[28:31], v[96:99], a[236:239]\n"
    "s_waitcnt vmcnt(11)\n"
    "v_mfma_f32_16x16x32_bf16 a[224:227], a[48:51], v[104:107], a[224:227]\n"
    "ds_write_b128 v47, v[224:227] offset:4096\n"
    "v_mfma_f32_16x16x32_bf16 a[224:227], a[64:67], v[108:111], a[224:227]\n"
    "buffer_load_dwordx4 v[84:87], v43, s[48:51], s69 offen offset:1024 sc1\n"
    "v_mfma_f32_16x16x32_bf16 a[228:231], a[68:71], v[108:111], a[228:231]\n"
    "ds_write_b128 v48, v[228:231] offset:4096\n"
    "v_mfma_f32_16x16x32_bf16 a[228:231], a[52:55], v[104:107], a[228:231]\n"
    "v_mfma_f32_16x16x32_bf16 a[232:235], a[56:59], v[104:107], a[232:235]\n"
    "ds_write_b128 v47, v[232:235] offset:4224\n"
    "v_mfma_f32_16x16x32_bf16 a[232:235], a[72:75], v[108:111], a[232:235]\n"
    "buffer_load_dwordx4 v[88:91], v43, s[48:51], s70 offen offset:1024 sc1\n"
    "v_mfma_f32_16x16x32_bf16 a[236:239], a[76:79], v[108:111], a[236:239]\n"
    "ds_write_b128 v48, v[236:239] offset:4224\n"
    "v_mfma_f32_16x16x32_bf16 a[236:239], a[60:63], v[104:107], a[236:239]\n"
    "s_waitcnt vmcnt(11)\n"
    "v_mfma_f32_16x16x32_bf16 a[224:227], a[80:83], v[112:115], a[224:227]\n"
    "v_mfma_f32_16x16x32_bf16 a[224:227], a[96:99], v[116:119], a[224:227]\n"
    "buffer_load_dwordx4 v[92:95], v43, s[48:51], s71 offen offset:1024 sc1\n"
    "v_mfma_f32_16x16x32_bf16 a[228:231], a[100:103], v[116:119], a[228:231]\n"
    "v_mfma_f32_16x16x32_bf16 a[228:231], a[84:87], v[112:115], a[228:231]\n"
    "v_mfma_f32_16x16x32_bf16 a[232:235], a[88:91], v[112:115], a[232:235]\n"
    "v_mfma_f32_16x16x32_bf16 a[232:235], a[104:107], v[116:119], a[232:235]\n"
    "buffer_load_dwordx4 v[96:99], v43, s[48:51], s72 offen offset:1024 sc1\n"
    "v_mfma_f32_16x16x32_bf16 a[236:239], a[108:111], v[116:119], a[236:239]\n"
    "v_mfma_f32_16x16x32_bf16 a[236:239], a[92:95], v[112:115], a[236:239]\n"
    "s_waitcnt vmcnt(11)\n"
    "v_mfma_f32_16x16x32_bf16 a[224:227], a[112:115], v[120:123], a[224:227]\n"
    "v_mfma_f32_16x16x32_bf16 a[224:227], a[128:131], v[124:127], a[224:227]\n"
    "buffer_load_dwordx4 v[100:103], v43, s[48:51], s73 offen offset:1024 sc1\n"
    "v_mfma_f32_16x16x32_bf16 a[228:231], a[132:135], v[124:127], a[228:231]\n"
    "v_mfma_f32_16x16x32_bf16 a[228:231], a[116:119], v[120:123], a[228:231]\n"
    "v_mfma_f32_16x16x32_bf16 a[232:235], a[120:123], v[120:123], a[232:235]\n"
    "v_mfma_f32_16x16x32_bf16 a[232:235], a[136:139], v[124:127], a[232:235]\n"
    "buffer_load_dwordx4 v[104:107], v43, s[48:51], s74 offen offset:1024 sc1\n"
    "v_mfma_f32_16x16x32_bf16 a[236:239], a[140:143], v[124:127], a[236:239]\n"
    "v_mfma_f32_16x16x32_bf16 a[236:239], a[124:127], v[120:123], a[236:239]\n"
    "s_waitcnt vmcnt(11)\n"
    "v_mfma_f32_16x16x32_bf16 a[224:227], a[144:147], v[128:131], a[224:227]\n"
    "v_mfma_f32_16x16x32_bf16 a[224:227], a[160:163], v[132:135], a[224:227]\n"
    "buffer_load_dwordx4 v[108:111], v43, s[48:51], s75 offen offset:1024 sc1\n"
    "v_mfma_f32_16x16x32_bf16 a[228:231], a[164:167], v[132:135], a[228:231]\n"
    "v_mfma_f32_16x16x32_bf16 a[228:231], a[148:151], v[128:131], a[228:231]\n"
    "v_mfma_f32_16x16x32_bf16 a[232:235], a[152:155], v[128:131], a[232:235]\n"
    "v_mfma_f32_16x16x32_bf16 a[232:235], a[168:171], v[132:135], a[232:235]\n"
    "buffer_load_dwordx4 v[112:115], v43, s[48:51], s76 offen offset:1024 sc1\n"
    "v_mfma_f32_16x16x32_bf16 a[236:239], a[172:175], v[132:135], a[236:239]\n"
    "v_mfma_f32_16x16x32_bf16 a[236:239], a[156:159], v[128:131], a[236:239]\n"
    "s_waitcnt vmcnt(11)\n"
    "v_mfma_f32_16x16x32_bf16 a[224:227], a[176:179], v[64:67], a[224:227]\n"
    "v_mfma_f32_16x16x32_bf16 a[224:227], a[192:195], v[68:71], a[224:227]\n"
    "buffer_load_dwordx4 v[116:119], v43, s[48:51], s77 offen offset:1024 sc1\n"
    "v_mfma_f32_16x16x32_bf16 a[228:231], a[196:199], v[68:71], a[228:231]\n"
    "v_mfma_f32_16x16x32_bf16 a[228:231], a[180:183], v[64:67], a[228:231]\n"
    "v_mfma_f32_16x16x32_bf16 a[232:235], a[184:187], v[64:67], a[232:235]\n"
    "v_mfma_f32_16x16x32_bf16 a[232:235], a[200:203], v[68:71], a[232:235]\n"
    "buffer_load_dwordx4 v[120:123], v43, s[48:51], s78 offen offset:1024 sc1\n"
    "v_mfma_f32_16x16x32_bf16 a[236:239], a[204:207], v[68:71], a[236:239]\n"
    "v_mfma_f32_16x16x32_bf16 a[236:239], a[188:191], v[64:67], a[236:239]\n"
    "s_waitcnt vmcnt(12)\n"
    "v_mfma_f32_16x16x32_bf16 a[224:227], a[208:211], v[72:75], a[224:227]\n"
    "v_mfma_f32_16x16x32_bf16 a[228:231], a[212:215], v[72:75], a[228:231]\n"
    "buffer_load_dwordx4 v[124:127], v43, s[48:51], s79 offen offset:1024 sc1\n"
    "s_mov_b64 exec, 0xff\n"
    "buffer_store_dwordx4 v[56:59], v63, s[48:51], s61 offen sc1\n"
    "s_mov_b64 exec, -1\n"
    "v_mfma_f32_16x16x32_bf16 a[232:235], a[216:219], v[72:75], a[232:235]\n"
    "v_mfma_f32_16x16x32_bf16 a[236:239], a[220:223], v[72:75], a[236:239]\n"
    "s_waitcnt vmcnt(12)\n"
    "v_mfma_f32_16x16x32_bf16 a[240:243], a[16:19], v[76:79], a[240:243]\n"
    "ds_write_b128 v47, a[224:227] offset:32768\n"
    "v_mfma_f32_16x16x32_bf16 a[240:243], a[32:35], v[80:83], a[240:243]\n"
    "v_mfma_f32_16x16x32_bf16 a[244:247], a[36:39], v[80:83], a[244:247]\n"
    "ds_write_b128 v48, a[228:231] offset:32768\n"
    "v_mfma_f32_16x16x32_bf16 a[244:247], a[20:23], v[76:79], a[244:247]\n"
    "v_mfma_f32_16x16x32_bf16 a[248:251], a[24:27], v[76:79], a[248:251]\n"
    "ds_write_b128 v47, a[232:235] offset:32896\n"
    "v_mfma_f32_16x16x32_bf16 a[248:251], a[40:43], v[80:83], a[248:251]\n"
    "v_mfma_f32_16x16x32_bf16 a[252:255], a[44:47], v[80:83], a[252:255]\n"
    "ds_write_b128 v48, a[236:239] offset:32896\n"
    "v_mfma_f32_16x16x32_bf16 a[252:255], a[28:31], v[76:79], a[252:255]\n"
    "s_waitcnt vmcnt(10)\n"
    "v_mfma_f32_16x16x32_bf16 a[240:243], a[48:51], v[84:87], a[240:243]\n"
    "v_mfma_f32_16x16x32_bf16 a[240:243], a[64:67], v[88:91], a[240:243]\n"
    "v_mfma_f32_16x16x32_bf16 a[244:247], a[68:71], v[88:91], a[244:247]\n"
    "v_mfma_f32_16x16x32_bf16 a[244:247], a[52:55], v[84:87], a[244:247]\n"
    "v_mfma_f32_16x16x32_bf16 a[248:251], a[56:59], v[84:87], a[248:251]\n"
    "v_mfma_f32_16x16x32_bf16 a[248:251], a[72:75], v[88:91], a[248:251]\n"
    "v_mfma_f32_16x16x32_bf16 a[252:255], a[76:79], v[88:91], a[252:255]\n"
    "v_mfma_f32_16x16x32_bf16 a[252:255], a[60:63], v[84:87], a[252:255]\n"
    "s_waitcnt lgkmcnt(0)\n"
    "s_barrier\n"
    "s_waitcnt vmcnt(8)\n"
    "v_mfma_f32_16x16x32_bf16 a[240:243], a[80:83], v[92:95], a[240:243]\n"
    "ds_read_b128 v[136:139], v49 offset:0\n"
    "v_mfma_f32_16x16x32_bf16 a[240:243], a[96:99], v[96:99], a[240:243]\n"
    "ds_read_b128 v[140:143], v50 offset:0\n"
    "v_mfma_f32_16x16x32_bf16 a[244:247], a[100:103], v[96:99], a[244:247]\n"
    "ds_read_b128 v[144:147], v49 offset:8192\n"
    "v_mfma_f32_16x16x32_bf16 a[244:247], a[84:87], v[92:95], a[244:247]\n"
    "ds_read_b128 v[148:151], v50 offset:8192\n"
    "v_mfma_f32_16x16x32_bf16 a[248:251], a[88:91], v[92:95], a[248:251]\n"
    "ds_read_b128 v[152:155], v49 offset:16384\n"
    "v_mfma_f32_16x16x32_bf16 a[248:251], a[104:107], v[96:99], a[248:251]\n"
    "ds_read_b128 v[156:159], v50 offset:16384\n"
    "v_mfma_f32_16x16x32_bf16 a[252:255], a[108:111], v[96:99], a[252:255]\n"
    "ds_read_b128 v[160:163], v49 offset:24576\n"
    "v_mfma_f32_16x16x32_bf16 a[252:255], a[92:95], v[92:95], a[252:255]\n"
    "ds_read_b128 v[164:167], v50 offset:24576\n"
    "s_waitcnt vmcnt(6)\n"
    "v_mfma_f32_16x16x32_bf16 a[240:243], a[112:115], v[100:103], a[240:243]\n"
    "ds_read_b128 v[168:171], v51 offset:32768\n"
    "v_mfma_f32_16x16x32_bf16 a[240:243], a[128:131], v[104:107], a[240:243]\n"
    "ds_read_b128 v[172:175], v51 offset:40960\n"
    "v_mfma_f32_16x16x32_bf16 a[244:247], a[132:135], v[104:107], a[244:247]\n"
    "ds_read_b128 v[176:179], v51 offset:49152\n"
    "v_mfma_f32_16x16x32_bf16 a[244:247], a[116:119], v[100:103], a[244:247]\n"
    "ds_read_b128 v[180:183], v51 offset:57344\n"
    "v_mfma_f32_16x16x32_bf16 a[248:251], a[120:123], v[100:103], a[248:251]\n"
    "v_mfma_f32_16x16x32_bf16 a[248:251], a[136:139], v[104:107], a[248:251]\n"
    "v_mfma_f32_16x16x32_bf16 a[252:255], a[140:143], v[104:107], a[252:255]\n"
    "v_mfma_f32_16x16x32_bf16 a[252:255], a[124:127], v[100:103], a[252:255]\n"
    "s_waitcnt vmcnt(4)\n"
    "v_mfma_f32_16x16x32_bf16 a[240:243], a[144:147], v[108:111], a[240:243]\n"
    "s_waitcnt lgkmcnt(4)\n"
    "v_add_f32 v136, v136, v144\n"
    "v_add_f32 v137, v137, v145\n"
    "v_mfma_f32_16x16x32_bf16 a[240:243], a[160:163], v[112:115], a[240:243]\n"
    "v_add_f32 v138, v138, v146\n"
    "v_add_f32 v139, v139, v147\n"
    "v_mfma_f32_16x16x32_bf16 a[244:247], a[164:167], v[112:115], a[244:247]\n"
    "v_add_f32 v140, v140, v148\n"
    "v_add_f32 v141, v141, v149\n"
    "v_mfma_f32_16x16x32_bf16 a[244:247], a[148:151], v[108:111], a[244:247]\n"
    "v_add_f32 v142, v142, v150\n"
    "v_add_f32 v143, v143, v151\n"
    "v_mfma_f32_16x16x32_bf16 a[248:251], a[152:155], v[108:111], a[248:251]\n"
    "v_add_f32 v136, v136, v152\n"
    "v_add_f32 v137, v137, v153\n"
    "v_mfma_f32_16x16x32_bf16 a[248:251], a[168:171], v[112:115], a[248:251]\n"
    "v_add_f32 v138, v138, v154\n"
    "v_add_f32 v139, v139, v155\n"
    "v_mfma_f32_16x16x32_bf16 a[252:255], a[172:175], v[112:115], a[252:255]\n"
    "v_add_f32 v140, v140, v156\n"
    "v_add_f32 v141, v141, v157\n"
    "v_mfma_f32_16x16x32_bf16 a[252:255], a[156:159], v[108:111], a[252:255]\n"
    "v_add_f32 v142, v142, v158\n"
    "s_waitcnt vmcnt(3)\n"
    "v_mfma_f32_16x16x32_bf16 a[240:243], a[176:179], v[116:119], a[240:243]\n"
    "v_add_f32 v143, v143, v159\n"
    "v_add_f32 v136, v136, v160\n"
    "v_mfma_f32_16x16x32_bf16 a[244:247], a[180:183], v[116:119], a[244:247]\n"
    "v_add_f32 v137, v137, v161\n"
    "v_add_f32 v138, v138, v162\n"
    "v_mfma_f32_16x16x32_bf16 a[248:251], a[184:187], v[116:119], a[248:251]\n"
    "v_add_f32 v139, v139, v163\n"
    "v_add_f32 v140, v140, v164\n"
    "v_mfma_f32_16x16x32_bf16 a[252:255], a[188:191], v[116:119], a[252:255]\n"
    "v_add_f32 v141, v141, v165\n"
    "s_waitcnt vmcnt(2)\n"
    "v_mfma_f32_16x16x32_bf16 a[240:243], a[192:195], v[120:123], a[240:243]\n"
    "v_add_f32 v142, v142, v166\n"
    "v_add_f32 v143, v143, v167\n"
    "v_mfma_f32_16x16x32_bf16 a[244:247], a[196:199], v[120:123], a[244:247]\n"
    "v_cvt_pk_bf16_f32 v144, v136, v137\n"
    "v_cvt_pk_bf16_f32 v145, v138, v139\n"
    "v_mfma_f32_16x16x32_bf16 a[248:251], a[200:203], v[120:123], a[248:251]\n"
    "v_cvt_pk_bf16_f32 v146, v140, v141\n"
    "v_cvt_pk_bf16_f32 v147, v142, v143\n"
    "v_mfma_f32_16x16x32_bf16 a[252:255], a[204:207], v[120:123], a[252:255]\n"
    "buffer_store_dwordx4 v[144:147], v52, s[52:55], s63 offen sc1\n"
    "s_add_u32 s63, s63, 28672\n"
    "s_waitcnt vmcnt(2)\n"
    "v_mfma_f32_16x16x32_bf16 a[240:243], a[208:211], v[124:127], a[240:243]\n"
    "s_waitcnt lgkmcnt(0)\n"
    "v_add_f32 v168, v168, v172\n"
    "v_add_f32 v169, v169, v173\n"
    "v_mfma_f32_16x16x32_bf16 a[244:247], a[212:215], v[124:127], a[244:247]\n"
    "v_add_f32 v170, v170, v174\n"
    "v_add_f32 v171, v171, v175\n"
    "v_mfma_f32_16x16x32_bf16 a[248:251], a[216:219], v[124:127], a[248:251]\n"
    "v_add_f32 v168, v168, v176\n"
    "v_add_f32 v169, v169, v177\n"
    "v_mfma_f32_16x16x32_bf16 a[252:255], a[220:223], v[124:127], a[252:255]\n"
    "v_add_f32 v170, v170, v178\n"
    "; ---- phase 2 MFMAs done\n"
    "v_add_f32 v171, v171, v179\n"
    "v_add_f32 v168, v168, v180\n"
    "v_add_f32 v169, v169, v181\n"
    "v_add_f32 v170, v170, v182\n"
    "v_add_f32 v171, v171, v183\n"
    "v_cvt_pk_bf16_f32 v172, v168, v169\n"
    "v_cvt_pk_bf16_f32 v173, v170, v171\n"
    "buffer_store_dwordx2 v[172:173], v53, s[52:55], s65 offen sc1\n"
    "s_add_u32 s65, s65, 14336\n"
    "ds_write_b128 v47, a[240:243] offset:36864\n"
    "ds_write_b128 v48, a[244:247] offset:36864\n"
    "ds_write_b128 v47, a[248:251] offset:36992\n"
    "ds_write_b128 v48, a[252:255] offset:36992\n"
    "s_waitcnt lgkmcnt(0)\n"
    "s_barrier\n"
    "; ---- last 16 rows: cross-wave sum, store\n"
    "ds_read_b128 v[184:187], v51 offset:36864\n"
    "ds_read_b128 v[188:191], v51 offset:45056\n"
    "ds_read_b128 v[192:195], v51 offset:53248\n"
    "ds_read_b128 v[196:199], v51 offset:61440\n"
    "s_waitcnt lgkmcnt(2)\n"
    "v_pk_add_f32 v[184:185], v[184:185], v[188:189]\n"
    "v_pk_add_f32 v[186:187], v[186:187], v[190:191]\n"
    "s_waitcnt lgkmcnt(1)\n"
    "v_pk_add_f32 v[184:185], v[184:185], v[192:193]\n"
    "v_pk_add_f32 v[186:187], v[186:187], v[194:195]\n"
    "s_waitcnt lgkmcnt(0)\n"
    "v_pk_add_f32 v[184:185], v[184:185], v[196:197]\n"
    "v_pk_add_f32 v[186:187], v[186:187], v[198:199]\n"
    "v_cvt_pk_bf16_f32 v188, v184, v185\n"
    "v_cvt_pk_bf16_f32 v189, v186, v187\n"
    "buffer_store_dwordx2 v[188:189], v53, s[52:55], s65 offen\n"
    "s_endpgm\n"
    ".Lend%=:\n"
    ".p2align 12\n"
    :
    : "s"(__builtin_amdgcn_kernarg_segment_ptr()), "s"(blockIdx.x), "s"(blockIdx.y), "s"(blockIdx.z), "v"(threadIdx.x),
      "s"((unsigned)(size_t)(__attribute__((address_space(3))) char*)smem)
    : "memory", "vcc", "scc", "s6", "s7", "s8", "s9", "s10", "s11", "s12", "s13", "s14", "s15", "s16", "s17", "s18", "s19", "s20", "s21", "s22", "s23", "s24", "s25", "s26",
      "s27", "s28", "s29", "s30", "s31", "s36", "s37", "s38", "s39", "s40", "s41", "s42", "s43", "s44", "s45", "s46", "s47", "s48", "s49", "s50", "s51", "s52", "s53", "s54",
      "s55", "s56", "s57", "s58", "s59", "s60", "s61", "s62", "s63", "s64", "s65", "s66", "s67", "s68", "s69", "s70", "s71", "s72", "s73", "s74", "s75", "s76", "s77", "s78",
      "s79", "s80", "s81", "s82", "s83", "s84", "s85", "s86", "s87", "s88", "s89", "s90", "s91", "s92", "s93", "s94", "s95", "s96", "s97", "s98", "s99", "v1", "v2", "v3",
      "v4", "v5", "v6", "v7", "v8", "v9", "v10", "v11", "v12", "v13", "v14", "v15", "v16", "v17", "v18", "v19", "v20", "v21", "v22", "v23", "v24", "v25", "v26", "v27",
      "v28", "v29", "v30", "v31", "v32", "v33", "v34", "v35", "v36", "v37", "v38", "v39", "v40", "v41", "v42", "v43", "v44", "v45", "v46", "v47", "v48", "v49", "v50", "v51",
      "v52", "v53", "v54", "v55", "v56", "v57", "v58", "v59", "v60", "v61", "v62", "v63", "v64", "v65", "v66", "v67", "v68", "v69", "v70", "v71", "v72", "v73", "v74", "v75",
      "v76", "v77", "v78", "v79", "v80", "v81", "v82", "v83", "v84", "v85", "v86", "v87", "v88", "v89", "v90", "v91", "v92", "v93", "v94", "v95", "v96", "v97", "v98", "v99",
      "v100", "v101", "v102", "v103", "v104", "v105", "v106", "v107", "v108", "v109", "v110", "v111", "v112", "v113", "v114", "v115", "v116", "v117", "v118", "v119", "v120", "v121", "v122", "v123",
      "v124", "v125", "v126", "v127", "v128", "v129", "v130", "v131", "v132", "v133", "v134", "v135", "v136", "v137", "v138", "v139", "v140", "v141", "v142", "v143", "v144", "v145", "v146", "v147",
      "v148", "v149", "v150", "v151", "v152", "v153", "v154", "v155", "v156", "v157", "v158", "v159", "v160", "v161", "v162", "v163", "v164", "v165", "v166", "v167", "v168", "v169", "v170", "v171",
      "v172", "v173", "v174", "v175", "v176", "v177", "v178", "v179", "v180", "v181", "v182", "v183", "v184", "v185", "v186", "v187", "v188", "v189", "v190", "v191", "v192", "v193", "v194", "v195",
      "v196", "v197", "v198", "v199", "v200", "v201", "v202", "v203", "v204", "v205", "v206", "v207", "v208", "v209", "v210", "v211", "v212", "v213", "v214", "v215", "v216", "v217", "v218", "v219",
      "v220", "v221", "v222", "v223", "v224", "v225", "v226", "v227", "v228", "v229", "v230", "v231", "v232", "v233", "v234", "v235", "v236", "v237", "v238", "v239", "v240", "v241", "v242", "v243",
      "v244", "v245", "v246", "v247", "v248", "v249", "v250", "v251", "v252", "v253", "v254", "v255", "a0", "a1", "a2", "a3", "a4", "a5", "a6", "a7", "a8", "a9", "a10", "a11",
      "a12", "a13", "a14", "a15", "a16", "a17", "a18", "a19", "a20", "a21", "a22", "a23", "a24", "a25", "a26", "a27", "a28", "a29", "a30", "a31", "a32", "a33", "a34", "a35",
      "a36", "a37", "a38", "a39", "a40", "a41", "a42", "a43", "a44", "a45", "a46", "a47", "a48", "a49", "a50", "a51", "a52", "a53", "a54", "a55", "a56", "a57", "a58", "a59",
      "a60", "a61", "a62", "a63", "a64", "a65", "a66", "a67", "a68", "a69", "a70", "a71", "a72", "a73", "a74", "a75", "a76", "a77", "a78", "a79", "a80", "a81", "a82", "a83",
      "a84", "a85", "a86", "a87", "a88", "a89", "a90", "a91", "a92", "a93", "a94", "a95", "a96", "a97", "a98", "a99", "a100", "a101", "a102", "a103", "a104", "a105", "a106", "a107",
      "a108", "a109", "a110", "a111", "a112", "a113", "a114", "a115", "a116", "a117", "a118", "a119", "a120", "a121", "a122", "a123", "a124", "a125", "a126", "a127", "a128", "a129", "a130", "a131",
      "a132", "a133", "a134", "a135", "a136", "a137", "a138", "a139", "a140", "a141", "a142", "a143", "a144", "a145", "a146", "a147", "a148", "a149", "a150", "a151", "a152", "a153", "a154", "a155",
      "a156", "a157", "a158", "a159", "a160", "a161", "a162", "a163", "a164", "a165", "a166", "a167", "a168", "a169", "a170", "a171", "a172", "a173", "a174", "a175", "a176", "a177", "a178", "a179",
      "a180", "a181", "a182", "a183", "a184", "a185", "a186", "a187", "a188", "a189", "a190", "a191", "a192", "a193", "a194", "a195", "a196", "a197", "a198", "a199", "a200", "a201", "a202", "a203",
      "a204", "a205", "a206", "a207", "a208", "a209", "a210", "a211", "a212", "a213", "a214", "a215", "a216", "a217", "a218", "a219", "a220", "a221", "a222", "a223", "a224", "a225", "a226", "a227",
      "a228", "a229", "a230", "a231", "a232", "a233", "a234", "a235", "a236", "a237", "a238", "a239", "a240", "a241", "a242", "a243", "a244", "a245", "a246", "a247", "a248", "a249", "a250", "a251",
      "a252", "a253", "a254", "a255");
}

extern "C" void kernel_launch(void* const* d_in, const int* in_sizes, int n_in, void* d_out, int out_size, void* d_ws,
                              size_t ws_size, hipStream_t stream) {
  const unsigned fmt = 0x00020000u;
  ffn_fused_kernel<<<dim3(8, 4, 8), 256, 0, stream>>>(d_in[0], 1835008u, fmt, d_in[1], 1605632u, fmt, d_in[2], 1605632u, fmt, d_ws, 13369344u, fmt, d_out, 1835008u, fmt);
}
```
